```python
import torch

def _softmax(x, axis=-1):
    return torch.softmax(x, dim=axis)
def _gelu(x):
    return torch.nn.functional.gelu(x, approximate="tanh")
def _silu(x):
    return torch.nn.functional.silu(x)
def _sigmoid(x):
    return torch.sigmoid(x)
def _layernorm(x, axis=-1, eps=1e-5):
    m = x.mean(dim=axis, keepdim=True)
    v = x.var(dim=axis, keepdim=True, unbiased=False)
    return (x - m) / torch.sqrt(v + eps)
def _scatter_add(tgt, idx, src, axis=0):
    return tgt.clone().index_add_(axis, idx, src)
def _segment_sum(src, seg, n):
    out = torch.zeros((n,) + src.shape[1:], dtype=src.dtype)
    return out.index_add_(0, seg, src)
def _pad(x, pw):
    flat = tuple(v for pair in reversed(pw) for v in pair)
    return torch.nn.functional.pad(x, flat)

kernel_name = 'recurrent_scan__ssm_conv_s5__mixer_s_mlp__prompt'

def setup_inputs():

    _w = (lambda a: (lambda t: t.to(torch.bfloat16) if t.is_floating_point() else t)(torch.as_tensor(a)))
    def _A_setup():
        B, S, D = 1, 2048, 640
        P = D
        Lambda_re = -0.5 * (lambda _x: torch.exp(torch.as_tensor(_x)))((lambda *s: torch.randn(*s, dtype=torch.bfloat16) * 0.2)(P))
        Lambda_im = (lambda *s: torch.randn(*s, dtype=torch.bfloat16) * 0.4)(P) * 1.0
        B_re = (lambda *s: torch.randn(*s, dtype=torch.bfloat16) * 0.2)(P, D) * (D ** -0.5)
        B_im = (lambda *s: torch.randn(*s, dtype=torch.bfloat16) * 0.2)(P, D) * (D ** -0.5)
        C_re = (lambda *s: torch.randn(*s, dtype=torch.bfloat16) * 0.2)(D, P) * (P ** -0.5)
        C_im = (lambda *s: torch.randn(*s, dtype=torch.bfloat16) * 0.2)(D, P) * (P ** -0.5)
        log_dt = (lambda *s: torch.randn(*s, dtype=torch.bfloat16) * 0.2)(P) * 0.3 - 2.0
        Dskip = (lambda *s: torch.randn(*s, dtype=torch.bfloat16) * 0.05)(D)
        return {
            "x": _w((lambda *s: torch.randn(*s, dtype=torch.bfloat16) * 0.05)(B, S, D)),
            "Lambda_re": _w(Lambda_re),
            "Lambda_im": _w(Lambda_im),
            "B_re": _w(B_re),
            "B_im": _w(B_im),
            "C_re": _w(C_re),
            "C_im": _w(C_im),
            "log_dt": _w(log_dt),
            "Dskip": _w(Dskip),
        }
    _A_in = _A_setup()

    def _B_setup():
        B, S, D, F = 1, 2048, 640, 1536
        return {
            "x":  _w((lambda *s: torch.randn(*s, dtype=torch.bfloat16) * 0.05)(B, S, D)),
            "w1": _w((lambda *s: torch.randn(*s, dtype=torch.bfloat16) * 3.2)(D, D) * (D ** -0.5)),
            "w2": _w((lambda *s: torch.randn(*s, dtype=torch.bfloat16) * 3.2)(D, D) * (D ** -0.5)),
            "w3": _w((lambda *s: torch.randn(*s, dtype=torch.bfloat16) * 3.2)(D, F) * (D ** -0.5)),
            "w4": _w((lambda *s: torch.randn(*s, dtype=torch.bfloat16) * 1.6)(F, D) * (F ** -0.5)),
        }
    _B_in = _B_setup()

    return {
        "A_x": _A_in["x"],
        "A_Lambda_re": _A_in["Lambda_re"],
        "A_Lambda_im": _A_in["Lambda_im"],
        "A_B_re": _A_in["B_re"],
        "A_B_im": _A_in["B_im"],
        "A_C_re": _A_in["C_re"],
        "A_C_im": _A_in["C_im"],
        "A_log_dt": _A_in["log_dt"],
        "A_Dskip": _A_in["Dskip"],
        "B_x": _B_in["x"],
        "B_w1": _B_in["w1"],
        "B_w2": _B_in["w2"],
        "B_w3": _B_in["w3"],
        "B_w4": _B_in["w4"],
    }

def reference(A_x, A_Lambda_re, A_Lambda_im, A_B_re, A_B_im, A_C_re, A_C_im, A_log_dt, A_Dskip, B_x, B_w1, B_w2, B_w3, B_w4):
    def _A_ref(x, Lambda_re, Lambda_im, B_re, B_im, C_re, C_im, log_dt, Dskip):
        Bsz, L, H = x.shape
        Lam = Lambda_re + 1j * Lambda_im
        Bmat = B_re + 1j * B_im
        Cmat = C_re + 1j * C_im
        dt = torch.exp(log_dt)
        Lam_bar = torch.exp(dt * Lam)
        B_bar = ((Lam_bar - 1.0) / Lam)[:, None] * Bmat
        Bu = torch.einsum("bld,pd->blp", x + 0j, B_bar)
        h = torch.zeros_like(Bu[:, 0, :])
        hs = []
        for t in range(L):
            h = Lam_bar[None, :] * h + Bu[:, t, :]
            hs.append(h)
        hseq = torch.stack(hs, 1)
        y = 2.0 * torch.einsum("blp,dp->bld", hseq, Cmat).real + x * Dskip[None, None, :]
        return y

    def _B_ref(x, w1, w2, w3, w4):
        B, S, D = x.shape
        h = _gelu(_layernorm(x, axis=-1) @ w1)
        c = D // 4
        g0 = _pad(h[:, :, 0*c:1*c], ((0, 0), (1, 0), (0, 0)))[:, :S, :]
        g1 = _pad(h[:, :, 1*c:2*c], ((0, 0), (0, 1), (0, 0)))[:, 1:, :]
        g2 = _pad(h[:, :, 2*c:3*c], ((0, 0), (2, 0), (0, 0)))[:, :S, :]
        g3 = _pad(h[:, :, 3*c:   ], ((0, 0), (0, 2), (0, 0)))[:, 2:, :]
        hs = torch.concatenate([g0, g1, g2, g3], axis=-1) @ w2
        x1 = x + hs
        h2 = _gelu(_layernorm(x1, axis=-1) @ w3) @ w4
        return x1 + h2

    x = A_x
    xn = _layernorm(x, axis=-1)
    s = _A_ref(xn, A_Lambda_re, A_Lambda_im, A_B_re, A_B_im, A_C_re, A_C_im, A_log_dt, A_Dskip)
    g = _silu(_B_ref(xn, B_w1, B_w2, B_w3, B_w4))
    return x + s * g
```

```cpp
#include <hip/hip_runtime.h>
#include <hip/hip_bf16.h>
#define MIMIC 1
typedef unsigned short u16;
constexpr int S=2048, D=640, P=640, F=1536;
__device__ __forceinline__ float b2f(u16 v){ return __uint_as_float(((unsigned)v)<<16); }
__device__ __forceinline__ u16 f2b(float f){ unsigned u=__float_as_uint(f); u += 0x7fff + ((u>>16)&1); return (u16)(u>>16); }
__device__ __forceinline__ float rb(float f){
  return b2f(f2b(f));
}
__global__ void k_cvt(const u16* x, float* o, int n){ int i=blockIdx.x*256+threadIdx.x; if(i<n) o[i]=b2f(x[i]); }
__global__ void k_ln(const float* x, float* o){
  int r=blockIdx.x; __shared__ float red[256];
  float s=0; for(int i=threadIdx.x;i<D;i+=256) s+=x[r*D+i];
  red[threadIdx.x]=s; __syncthreads();
  for(int k=128;k>0;k>>=1){ if(threadIdx.x<k) red[threadIdx.x]+=red[threadIdx.x+k]; __syncthreads(); }
  float m=red[0]/D; __syncthreads();
  float v=0; for(int i=threadIdx.x;i<D;i+=256){ float d=x[r*D+i]-m; v+=d*d; }
  red[threadIdx.x]=v; __syncthreads();
  for(int k=128;k>0;k>>=1){ if(threadIdx.x<k) red[threadIdx.x]+=red[threadIdx.x+k]; __syncthreads(); }
  float var=red[0]/D;
  float mb=rb(m), vb=rb(var);
  float den=rb(sqrtf(rb(vb+1e-5f)));
  for(int i=threadIdx.x;i<D;i+=256) o[r*D+i]=rb(rb(x[r*D+i]-mb)/den);
}
__global__ void k_gemm(const float* A, const u16* B, float* C, int N, int K, int tb, int ldc){
  __shared__ float As[16][17], Bs[16][17];
  int tx=threadIdx.x&15, ty=threadIdx.x>>4;
  int m=blockIdx.y*16+ty, n=blockIdx.x*16+tx;
  float acc=0;
  for(int k0=0;k0<K;k0+=16){
    As[ty][tx]=A[m*K+k0+tx];
    if(tb) Bs[tx][ty]=b2f(B[(blockIdx.x*16+ty)*K+k0+tx]); else Bs[ty][tx]=b2f(B[(k0+ty)*N+n]);
    __syncthreads();
    for(int k=0;k<16;k++) acc+=As[ty][k]*Bs[k][tx];
    __syncthreads();
  }
  C[m*ldc+n]=acc;
}
__device__ __forceinline__ float gelu(float x){ float u=0.7978845608028654f*(x+0.044715f*x*x*x); return 0.5f*x*(1.f+tanhf(u)); }
__global__ void k_gelu(float* a, int n){ int i=blockIdx.x*256+threadIdx.x; if(i<n) a[i]=rb(gelu(rb(a[i]))); }
__global__ void k_shift(const float* h, float* o){ int i=blockIdx.x*256+threadIdx.x; int r=i/D,c=i%D; int g=c/160; int sh=(g==0)?-1:(g==1)?1:(g==2)?-2:2; int rr=r+sh; o[i]=(rr>=0&&rr<S)?h[rr*D+c]:0.f; }
__global__ void k_add(const float* a, const float* b, float* o, int n){ int i=blockIdx.x*256+threadIdx.x; if(i<n) o[i]=rb(a[i]+rb(b[i])); }
__global__ void k_scan(const float* bre, const float* bim, const u16* lre, const u16* lim, const u16* ldt, float* hre, float* him){
  int p=blockIdx.x*64+threadIdx.x;
  float dt=rb(expf(b2f(ldt[p]))); float a=b2f(lre[p]), b=b2f(lim[p]);
  float e=expf(dt*a); float lr=e*cosf(dt*b), li=e*sinf(dt*b);
  float nr=lr-1.f, ni=li; float den=a*a+b*b; float cr=(nr*a+ni*b)/den, ci=(ni*a-nr*b)/den;
  float hr=0,hi=0;
  for(int t=0;t<S;t++){ float ur=bre[t*P+p], ui=bim[t*P+p]; float br=cr*ur-ci*ui, bi=cr*ui+ci*ur;
    float tr=lr*hr-li*hi+br, ti=lr*hi+li*hr+bi; hr=tr; hi=ti; hre[t*P+p]=hr; him[t*P+p]=hi; }
}
__global__ void k_final(const float* x, const float* xn, const float* yre, const float* yim, const u16* dskip, const float* z, float* out){
  int i=blockIdx.x*256+threadIdx.x; int c=i%D;
  float s=2.f*(yre[i]-yim[i])+rb(xn[i]*b2f(dskip[c]));
  float zz=z[i]; float g=rb(zz/(1.f+expf(-zz)));
  out[i]=x[i]+s*g;
}
extern "C" void kernel_launch(void* const* d_in, const int* in_sizes, int n_in, void* d_out, int out_size, void* d_ws, size_t ws_size, hipStream_t stream){
  const u16* x=(const u16*)d_in[0]; const u16* lre=(const u16*)d_in[1]; const u16* lim=(const u16*)d_in[2];
  const u16* Bre=(const u16*)d_in[3]; const u16* Bim=(const u16*)d_in[4]; const u16* Cre=(const u16*)d_in[5]; const u16* Cim=(const u16*)d_in[6];
  const u16* ldt=(const u16*)d_in[7]; const u16* dsk=(const u16*)d_in[8];
  const u16* w1=(const u16*)d_in[10]; const u16* w2=(const u16*)d_in[11]; const u16* w3=(const u16*)d_in[12]; const u16* w4=(const u16*)d_in[13];
  float* ws=(float*)d_ws; size_t n=S*D;
  float *xf=ws, *xn=ws+n, *xnn=ws+2*n, *h1=ws+3*n, *hs=ws+4*n, *t=ws+5*n, *x1=ws+6*n, *x1n=ws+7*n, *z=ws+8*n,
   *bre=ws+9*n,*bim=ws+10*n,*hre=ws+11*n,*him=ws+12*n,*yre=ws+13*n,*yim=ws+14*n,*hid=ws+15*n;
  int nb=n/256;
  k_cvt<<<nb,256,0,stream>>>(x,xf,n);
  k_ln<<<S,256,0,stream>>>(xf,xn);
  k_ln<<<S,256,0,stream>>>(xn,xnn);
  k_gemm<<<dim3(D/16,S/16),256,0,stream>>>(xnn,w1,h1,D,D,0,D);
  k_gelu<<<nb,256,0,stream>>>(h1,n);
  k_shift<<<nb,256,0,stream>>>(h1,hs);
  k_gemm<<<dim3(D/16,S/16),256,0,stream>>>(hs,w2,t,D,D,0,D);
  k_add<<<nb,256,0,stream>>>(xn,t,x1,n);
  k_ln<<<S,256,0,stream>>>(x1,x1n);
  k_gemm<<<dim3(F/16,S/16),256,0,stream>>>(x1n,w3,hid,F,D,0,F);
  k_gelu<<<S*F/256,256,0,stream>>>(hid,S*F);
  k_gemm<<<dim3(D/16,S/16),256,0,stream>>>(hid,w4,t,D,F,0,D);
  k_add<<<nb,256,0,stream>>>(x1,t,z,n);
  k_gemm<<<dim3(P/16,S/16),256,0,stream>>>(xn,Bre,bre,P,D,1,P);
  k_gemm<<<dim3(P/16,S/16),256,0,stream>>>(xn,Bim,bim,P,D,1,P);
  k_scan<<<P/64,64,0,stream>>>(bre,bim,lre,lim,ldt,hre,him);
  k_gemm<<<dim3(D/16,S/16),256,0,stream>>>(hre,Cre,yre,D,P,1,D);
  k_gemm<<<dim3(D/16,S/16),256,0,stream>>>(him,Cim,yim,D,P,1,D);
  k_final<<<nb,256,0,stream>>>(xf,xn,yre,yim,dsk,z,(float*)d_out);
}
```

```cpp
#include <hip/hip_runtime.h>
#include <hip/hip_bf16.h>
#define MIMIC 1
typedef unsigned short u16;
constexpr int S=2048, D=640, P=640, F=1536;
__device__ __forceinline__ float b2f(u16 v){ return __uint_as_float(((unsigned)v)<<16); }
__device__ __forceinline__ u16 f2b(float f){ unsigned u=__float_as_uint(f); u += 0x7fff + ((u>>16)&1); return (u16)(u>>16); }
__device__ __forceinline__ float rb(float f){
  return b2f(f2b(f));
}
__global__ void k_cvt(const u16* x, float* o, int n){ int i=blockIdx.x*256+threadIdx.x; if(i<n) o[i]=b2f(x[i]); }
__global__ void k_ln(const float* x, float* o){
  int r=blockIdx.x; __shared__ float red[256];
  float s=0; for(int i=threadIdx.x;i<D;i+=256) s+=x[r*D+i];
  red[threadIdx.x]=s; __syncthreads();
  for(int k=128;k>0;k>>=1){ if(threadIdx.x<k) red[threadIdx.x]+=red[threadIdx.x+k]; __syncthreads(); }
  float m=red[0]/D; __syncthreads();
  float v=0; for(int i=threadIdx.x;i<D;i+=256){ float d=x[r*D+i]-m; v+=d*d; }
  red[threadIdx.x]=v; __syncthreads();
  for(int k=128;k>0;k>>=1){ if(threadIdx.x<k) red[threadIdx.x]+=red[threadIdx.x+k]; __syncthreads(); }
  float var=red[0]/D;
  float mb=rb(m), vb=rb(var);
  float den=rb(sqrtf(rb(vb+1e-5f)));
  for(int i=threadIdx.x;i<D;i+=256) o[r*D+i]=rb(rb(x[r*D+i]-mb)/den);
}
__global__ void k_gemm(const float* A, const u16* B, float* C, int N, int K, int tb, int ldc){
  __shared__ float As[16][17], Bs[16][17];
  int tx=threadIdx.x&15, ty=threadIdx.x>>4;
  int m=blockIdx.y*16+ty, n=blockIdx.x*16+tx;
  float acc=0;
  for(int k0=0;k0<K;k0+=16){
    As[ty][tx]=A[m*K+k0+tx];
    if(tb) Bs[tx][ty]=b2f(B[(blockIdx.x*16+ty)*K+k0+tx]); else Bs[ty][tx]=b2f(B[(k0+ty)*N+n]);
    __syncthreads();
    for(int k=0;k<16;k++) acc+=As[ty][k]*Bs[k][tx];
    __syncthreads();
  }
  C[m*ldc+n]=acc;
}
__device__ __forceinline__ float gelu(float x){ float u=0.7978845608028654f*(x+0.044715f*x*x*x); return 0.5f*x*(1.f+tanhf(u)); }
__global__ void k_gelu(float* a, int n){ int i=blockIdx.x*256+threadIdx.x; if(i<n) a[i]=rb(gelu(rb(a[i]))); }
__global__ void k_shift(const float* h, float* o){ int i=blockIdx.x*256+threadIdx.x; int r=i/D,c=i%D; int g=c/160; int sh=(g==0)?-1:(g==1)?1:(g==2)?-2:2; int rr=r+sh; o[i]=(rr>=0&&rr<S)?h[rr*D+c]:0.f; }
__global__ void k_add(const float* a, const float* b, float* o, int n){ int i=blockIdx.x*256+threadIdx.x; if(i<n) o[i]=rb(a[i]+rb(b[i])); }
__global__ void k_scan(const float* bre, const float* bim, const u16* lre, const u16* lim, const u16* ldt, float* hre, float* him){
  int p=blockIdx.x*64+threadIdx.x;
  float dt=rb(expf(b2f(ldt[p]))); float a=b2f(lre[p]), b=b2f(lim[p]);
  float e=expf(dt*a); float lr=e*cosf(dt*b), li=e*sinf(dt*b);
  float nr=lr-1.f, ni=li; float den=a*a+b*b; float cr=(nr*a+ni*b)/den, ci=(ni*a-nr*b)/den;
  float hr=0,hi=0;
  for(int t=0;t<S;t++){ float ur=bre[t*P+p], ui=bim[t*P+p]; float br=cr*ur-ci*ui, bi=cr*ui+ci*ur;
    float tr=lr*hr-li*hi+br, ti=lr*hi+li*hr+bi; hr=tr; hi=ti; hre[t*P+p]=hr; him[t*P+p]=hi; }
}
__global__ void k_final(const float* x, const float* xn, const float* yre, const float* yim, const u16* dskip, const float* z, float* out){
  int i=blockIdx.x*256+threadIdx.x; int c=i%D;
  float s=2.f*(yre[i]-yim[i])+rb(xn[i]*b2f(dskip[c]));
  float zz=z[i]; float g=rb(zz/(1.f+expf(-zz)));
  out[i]=x[i]+s*g;
}
__global__ void k_ic0(float* p){ float a=p[0], b=p[1]; _Pragma("nounroll") for(int i=0;i<64;i++){ asm volatile(".rept 128\n v_fma_f32 %0, %0, %1, %1\n .endr" : "+v"(a) : "v"(b)); } if(a==123.456f) p[2]=a; }
__global__ void k_ic1(float* p){ float a=p[0], b=p[1]; _Pragma("nounroll") for(int i=0;i<64;i++){ asm volatile(".rept 128\n v_fma_f32 %0, %0, %1, %1\n .endr" : "+v"(a) : "v"(b)); } if(a==123.456f) p[2]=a; }
__global__ void k_ic2(float* p){ float a=p[0], b=p[1]; _Pragma("nounroll") for(int i=0;i<64;i++){ asm volatile(".rept 128\n v_fma_f32 %0, %0, %1, %1\n .endr" : "+v"(a) : "v"(b)); } if(a==123.456f) p[2]=a; }
__global__ void k_ic3(float* p){ float a=p[0], b=p[1]; _Pragma("nounroll") for(int i=0;i<64;i++){ asm volatile(".rept 128\n v_fma_f32 %0, %0, %1, %1\n .endr" : "+v"(a) : "v"(b)); } if(a==123.456f) p[2]=a; }
__global__ void k_ic4(float* p){ float a=p[0], b=p[1]; _Pragma("nounroll") for(int i=0;i<64;i++){ asm volatile(".rept 128\n v_fma_f32 %0, %0, %1, %1\n .endr" : "+v"(a) : "v"(b)); } if(a==123.456f) p[2]=a; }
__global__ void k_ic5(float* p){ float a=p[0], b=p[1]; _Pragma("nounroll") for(int i=0;i<64;i++){ asm volatile(".rept 128\n v_fma_f32 %0, %0, %1, %1\n .endr" : "+v"(a) : "v"(b)); } if(a==123.456f) p[2]=a; }
__global__ void k_ic6(float* p){ float a=p[0], b=p[1]; _Pragma("nounroll") for(int i=0;i<64;i++){ asm volatile(".rept 128\n v_fma_f32 %0, %0, %1, %1\n .endr" : "+v"(a) : "v"(b)); } if(a==123.456f) p[2]=a; }
__global__ void k_ic7(float* p){ float a=p[0], b=p[1]; _Pragma("nounroll") for(int i=0;i<64;i++){ asm volatile(".rept 128\n v_fma_f32 %0, %0, %1, %1\n .endr" : "+v"(a) : "v"(b)); } if(a==123.456f) p[2]=a; }
extern "C" void kernel_launch(void* const* d_in, const int* in_sizes, int n_in, void* d_out, int out_size, void* d_ws, size_t ws_size, hipStream_t stream){
  const u16* x=(const u16*)d_in[0]; const u16* lre=(const u16*)d_in[1]; const u16* lim=(const u16*)d_in[2];
  const u16* Bre=(const u16*)d_in[3]; const u16* Bim=(const u16*)d_in[4]; const u16* Cre=(const u16*)d_in[5]; const u16* Cim=(const u16*)d_in[6];
  const u16* ldt=(const u16*)d_in[7]; const u16* dsk=(const u16*)d_in[8];
  const u16* w1=(const u16*)d_in[10]; const u16* w2=(const u16*)d_in[11]; const u16* w3=(const u16*)d_in[12]; const u16* w4=(const u16*)d_in[13];
  float* ws=(float*)d_ws; size_t n=S*D;
  float *xf=ws, *xn=ws+n, *xnn=ws+2*n, *h1=ws+3*n, *hs=ws+4*n, *t=ws+5*n, *x1=ws+6*n, *x1n=ws+7*n, *z=ws+8*n,
   *bre=ws+9*n,*bim=ws+10*n,*hre=ws+11*n,*him=ws+12*n,*yre=ws+13*n,*yim=ws+14*n,*hid=ws+15*n;
  int nb=n/256;
  k_cvt<<<nb,256,0,stream>>>(x,xf,n);
  k_ln<<<S,256,0,stream>>>(xf,xn);
  k_ln<<<S,256,0,stream>>>(xn,xnn);
  k_gemm<<<dim3(D/16,S/16),256,0,stream>>>(xnn,w1,h1,D,D,0,D);
  k_gelu<<<nb,256,0,stream>>>(h1,n);
  k_shift<<<nb,256,0,stream>>>(h1,hs);
  k_gemm<<<dim3(D/16,S/16),256,0,stream>>>(hs,w2,t,D,D,0,D);
  k_add<<<nb,256,0,stream>>>(xn,t,x1,n);
  k_ln<<<S,256,0,stream>>>(x1,x1n);
  k_gemm<<<dim3(F/16,S/16),256,0,stream>>>(x1n,w3,hid,F,D,0,F);
  k_gelu<<<S*F/256,256,0,stream>>>(hid,S*F);
  k_gemm<<<dim3(D/16,S/16),256,0,stream>>>(hid,w4,t,D,F,0,D);
  k_add<<<nb,256,0,stream>>>(x1,t,z,n);
  k_gemm<<<dim3(P/16,S/16),256,0,stream>>>(xn,Bre,bre,P,D,1,P);
  k_gemm<<<dim3(P/16,S/16),256,0,stream>>>(xn,Bim,bim,P,D,1,P);
  k_scan<<<P/64,64,0,stream>>>(bre,bim,lre,lim,ldt,hre,him);
  k_gemm<<<dim3(D/16,S/16),256,0,stream>>>(hre,Cre,yre,D,P,1,D);
  k_gemm<<<dim3(D/16,S/16),256,0,stream>>>(him,Cim,yim,D,P,1,D);
  k_ic0<<<256,64,0,stream>>>(ws+20*n);
  k_ic1<<<256,64,0,stream>>>(ws+20*n);
  k_ic2<<<256,64,0,stream>>>(ws+20*n);
  k_ic3<<<256,64,0,stream>>>(ws+20*n);
  k_ic4<<<256,64,0,stream>>>(ws+20*n);
  k_ic5<<<256,64,0,stream>>>(ws+20*n);
  k_ic6<<<256,64,0,stream>>>(ws+20*n);
  k_ic7<<<256,64,0,stream>>>(ws+20*n);
  k_final<<<nb,256,0,stream>>>(xf,xn,yre,yim,dsk,z,(float*)d_out);
}
```

```cpp
#include <hip/hip_runtime.h>
#include <hip/hip_bf16.h>
#include <type_traits>
#pragma clang diagnostic ignored "-Wint-to-pointer-cast"

typedef unsigned short u16;
typedef unsigned u32;
typedef unsigned long long u64;
typedef __attribute__((ext_vector_type(8))) short bf16x8;
typedef __attribute__((ext_vector_type(4))) short s16x4;
typedef __attribute__((ext_vector_type(4))) float f32x4;
typedef __attribute__((ext_vector_type(2))) float f32x2;
typedef __attribute__((ext_vector_type(4))) unsigned u32x4;
typedef __attribute__((ext_vector_type(2))) unsigned u32x2;
typedef __attribute__((ext_vector_type(2))) __bf16 bf2;
#define AS3 __attribute__((address_space(3)))
#define DEV __device__ __forceinline__

constexpr u32 W_EPOCH = 0, W_FRT = 32768, W_FH1 = 65536, W_FAGG = 98304, W_FX1 = 131072, W_FHH = 163840, W_FHD = 196608;
constexpr u32 W_AGG = 262144, W_ST = 524288, W_H1 = 1u << 20, W_X1 = 4u << 20, W_HH = 8u << 20, W_HD = 14u << 20;
constexpr u32 W_W2R = 21u << 20, W_W4R = 22u << 20, W_END = 24u << 20;
constexpr u32 SPIN_CAP = 1u << 17;

#define MAP 1
#define SW1 2u
#define SW3 0u
#define SW4 4u
#define SW5 4u
#define SW6 4u
#define LOADER_FIRST() do { L_X(0,0); } while(0)
#define LOADER_SECOND() do { L_W1(0,81920); } while(0)
#define LOADER_PROLOGUE() do { PREFETCH_1(); L_X(1,8192); L_W1(1,92160); } while(0)
#define LOADER_SCHEDULE() do { \
  LWAIT(6);  LBAR();   \
  L_X(2,16384); L_W1(2,102400); L_X(3,24576); L_W1(3,112640); LWAIT(10);  LBAR();   \
  L_X(4,32768); L_W1(4,122880); JOB_TAG(); LWAIT(10);  LBAR();   \
  L_X(5,40960); L_W1(5,133120); LWAIT(10);  LBAR();   \
  L_X(6,49152); L_W1(6,143360); LWAIT(10);  LBAR();   \
  L_X(7,57344); L_W1(7,81920); LWAIT(10);  LBAR();   \
  L_X(8,65536); L_W1(8,92160); LWAIT(10);  LBAR();   \
  L_X(9,73728); L_W1(9,102400); LWAIT(10);  LBAR();   \
  L_BM(0,122880); LWAIT(10);  LBAR();   \
  LWAIT(5);  LBAR();   \
  L_BM(1,81920); PREFETCH_2(); LBAR();   \
  L_BM(2,102400); LBAR();   \
  LWAIT(11);  LBAR();   \
  LWAIT(6);  LBAR();   \
  L_BM(3,122880); POLL_ISSUE_RT(); LWAIT(6);  LBAR();   \
  L_BM(4,81920); LWAIT(6);  LBAR();   \
  L_BM(5,102400); POLL_CHECK_RTB(10); LWAIT(5);  LBAR();   \
  L_BM(6,122880); L_W2(0,143360); LWAIT(8);  LBAR();   \
  L_BM(7,81920); LWAIT(8);  LBAR();   \
  POLL_ISSUE_H1(); L_BM(8,102400); LWAIT(6);  LBAR();   \
  L_BM(9,122880); POLL_CHECK_H1B(10); LWAIT(5);  LBAR();   \
  L_W2(1,81920); L_H1(0,0); LWAIT(5);  LBAR();   \
  L_H1(1,8192); L_W2(2,92160); LBAR();   \
  L_H1(2,16384); L_W2(3,102400); LBAR();   \
  L_H1(3,24576); L_W2(4,112640); L_H1(4,32768); L_W2(5,122880); L_H1(5,40960); LWAIT(22);  LBAR();   \
  L_W2(6,133120); L_H1(6,49152); L_H1(7,57344); LWAIT(27);  LBAR();   \
  L_W2(7,143360); LWAIT(25);  LBAR();   \
  L_W2(8,81920); LWAIT(23);  LBAR();   \
  L_H1(8,65536); L_H1(9,73728); LWAIT(22);  LBAR();   \
  L_W2(9,92160); LWAIT(20);  LBAR();   \
  R_W3A(0,"v[64:67]","v[68:71]","v[72:75]"); LWAIT(18);  LBAR();   \
  R_W3B(0,"v[76:79]","v[80:83]","v[84:87]"); LWAIT(16);  LBAR();   \
  POLL_ISSUE_AGG(); L_W3A(1,106496); LWAIT(15);  LBAR();   \
  L_W3B(1,106496); LWAIT(13);  LBAR();   \
  POLL_CHECK_AGGB(6); LBAR();   \
  L_AGG(0); LWAIT(12); W_W3(24576,"v[64:67]","v[68:71]","v[72:75]","v[76:79]","v[80:83]","v[84:87]"); LWAIT(0);  LGW(); LBAR();   \
  L_W3(2,131072); L_W3(3,81920); POLL_SPIN_X1(); L_X1(0,49152); L_ST(20480); L_X1(1,57344); L_X1(2,65536); L_X1(3,73728); LBAR();   \
  LWAIT(6);  LBAR();   \
  JOB_ST(); LWAIT(4);  LBAR();   \
  L_W3(4,24576); L_X1(4,0); LWAIT(10);  LBAR();   \
  L_W3(5,106496); L_X1(5,8192); LWAIT(16);  LBAR();   \
  L_W3(6,131072); L_X1(6,16384); LWAIT(16);  LBAR();   \
  L_W3(7,81920); L_X1(7,73728); LWAIT(16);  LBAR();   \
  L_W3(8,24576); L_X1(8,0); LWAIT(16);  LBAR();   \
  POLL_ISSUE_HH(); L_W3(9,106496); L_X1(9,8192); LWAIT(17);  LBAR();   \
  L_C(0,133120); L_C(1,143360); LWAIT(15);  LBAR();   \
  L_C(2,81920); POLL_CHECK_HHB(17); LWAIT(9);  LBAR();   \
  L_HH(0,49152); L_HH(1,57344); JOB_SU(); JOB_SV(); LBAR();   \
  L_HH(2,65536); L_C(3,92160); L_HH(3,16384); L_HH(4,73728); L_C(4,102400); L_HH(5,0); L_C(5,112640); LWAIT(19);  LBAR();   \
  L_HH(6,24576); L_C(6,122880); LWAIT(22);  LBAR();   \
  L_HH(7,32768); L_C(7,133120); LWAIT(25);  LBAR();   \
  L_HH(8,40960); L_C(8,143360); LWAIT(25);  LBAR();   \
  L_HH(9,8192); L_C(9,81920); LWAIT(25);  LBAR();   \
  L_HH(10,49152); L_C(10,92160); LWAIT(25);  LBAR();   \
  L_HH(11,57344); L_C(11,102400); LWAIT(25);  LBAR();   \
  L_HH(12,65536); L_C(12,112640); LWAIT(25);  LBAR();   \
  L_HH(13,16384); L_C(13,122880); LWAIT(25);  LBAR();   \
  L_HH(14,73728); L_C(14,133120); LWAIT(25);  LBAR();   \
  L_HH(15,0); L_C(15,143360); POLL_ISSUE_HD(); LWAIT(26);  LBAR();   \
  L_HH(16,24576); L_C(16,81920); LWAIT(26);  LBAR();   \
  L_HH(17,32768); L_C(17,92160); LWAIT(26);  LBAR();   \
  L_HH(18,40960); L_C(18,102400); LWAIT(26);  LBAR();   \
  L_HH(19,8192); L_C(19,112640); POLL_CHECK_HDB(20); LWAIT(26);  LBAR();   \
  L_HD(0,49152); L_W4(0,122880); LWAIT(26);  LBAR();   \
  L_HD(1,57344); L_W4(1,133120); LWAIT(25);  LBAR();   \
  L_HD(2,65536); L_W4(2,143360); LWAIT(25);  LBAR();   \
  L_HD(3,16384); L_W4(3,81920); LWAIT(25);  LBAR();   \
  L_HD(4,73728); L_W4(4,92160); LWAIT(25);  LBAR();   \
  L_HD(5,0); L_W4(5,102400); LWAIT(25);  LBAR();   \
  L_HD(6,24576); L_W4(6,112640); LWAIT(25);  LBAR();   \
  L_HD(7,32768); L_W4(7,122880); LWAIT(25);  LBAR();   \
  L_HD(8,40960); L_W4(8,133120); LWAIT(25);  LBAR();   \
  L_HD(9,8192); L_W4(9,143360); LWAIT(25);  LBAR();   \
  L_HD(10,49152); L_W4(10,81920); LWAIT(25);  LBAR();   \
  L_HD(11,57344); L_W4(11,92160); LWAIT(25);  LBAR();   \
  L_HD(12,65536); L_W4(12,102400); LWAIT(25);  LBAR();   \
  L_HD(13,16384); L_W4(13,112640); LWAIT(25);  LBAR();   \
  L_HD(14,73728); L_W4(14,122880); LWAIT(25);  LBAR();   \
  L_HD(15,0); L_W4(15,133120); LWAIT(25);  LBAR();   \
  L_HD(16,24576); L_W4(16,143360); LWAIT(25);  LBAR();   \
  L_HD(17,32768); L_W4(17,81920); LWAIT(25);  LBAR();   \
  LWAIT(20);  LBAR();   \
  L_HD(18,40960); L_W4(18,92160); LWAIT(20);  LBAR();   \
  LWAIT(15);  LBAR();   \
  L_HD(19,8192); L_W4(19,102400); LWAIT(15);  LBAR();   \
  LWAIT(10);  LBAR();   \
  L_HD(20,49152); L_W4(20,112640); LWAIT(10);  LBAR();   \
  L_HD(21,57344); L_W4(21,122880); LWAIT(10);  LBAR();   \
  L_HD(22,65536); L_W4(22,133120); LWAIT(10);  LBAR();   \
  L_HD(23,16384); L_W4(23,143360); LWAIT(10);  LBAR();   \
  LWAIT(5);  LBAR();   \
  LWAIT(0);  LBAR();   \
  } while(0)
constexpr int ASLOT[84]={0,8192,16384,24576,32768,40960,49152,57344,65536,73728,0,8192,16384,24576,32768,40960,49152,57344,65536,73728,0,8192,16384,24576,32768,40960,49152,57344,65536,73728,49152,57344,65536,73728,0,8192,16384,73728,0,8192,49152,57344,65536,16384,73728,0,24576,32768,40960,8192,49152,57344,65536,16384,73728,0,24576,32768,40960,8192,49152,57344,65536,16384,73728,0,24576,32768,40960,8192,49152,57344,65536,16384,73728,0,24576,32768,40960,8192,49152,57344,65536,16384};
constexpr int BSLOT[84]={81920,92160,102400,112640,122880,133120,143360,81920,92160,102400,122880,81920,102400,122880,81920,102400,122880,81920,102400,122880,143360,81920,92160,102400,112640,122880,133120,143360,81920,92160,24576,106496,131072,81920,24576,106496,131072,81920,24576,106496,133120,143360,81920,92160,102400,112640,122880,133120,143360,81920,92160,102400,112640,122880,133120,143360,81920,92160,102400,112640,122880,133120,143360,81920,92160,102400,112640,122880,133120,143360,81920,92160,102400,112640,122880,133120,143360,81920,92160,102400,112640,122880,133120,143360};
constexpr int L_AGGO=0, L_STO=20480, L_GAP=153600, L_MISC=155648;
constexpr int NL=0;
constexpr int L_TAB = L_MISC, L_STATS = L_MISC + 6400, L_SYNC = L_MISC + 6912, L_DSK = L_MISC + 6928, L_DUMMY = L_MISC + 7424, L_ST2 = L_MISC, L_CS = L_GAP, L_TOT = L_GAP + 1280;

template <int I, int N, class F> DEV void sfor(F&& f) {
  if constexpr (I < N) { f(std::integral_constant<int, I>{}); sfor<I + 1, N>(f); }
}
DEV bf16x8 ld128(u32 a) { return *(AS3 const bf16x8*)a; }
DEV f32x4 ldf4(u32 a) { return *(AS3 const f32x4*)a; }
DEV f32x2 ldf2(u32 a) { return *(AS3 const f32x2*)a; }
DEV float ldf1(u32 a) { return *(AS3 const float*)a; }
DEV u32x2 ldu2(u32 a) { return *(AS3 const u32x2*)a; }
DEV bf16x8 ldtr(u32 a0, u32 a1) {
  s16x4 lo = __builtin_amdgcn_ds_read_tr16_b64_v4i16((AS3 s16x4*)a0);
  s16x4 hi = __builtin_amdgcn_ds_read_tr16_b64_v4i16((AS3 s16x4*)a1);
  return __builtin_shufflevector(lo, hi, 0, 1, 2, 3, 4, 5, 6, 7);
}
struct LA {
  u32 b0, b1, b2;
  DEV LA(u32 b) : b0(b) {
    asm("v_add_u32 %0, 0x10000, %1" : "=v"(b1) : "v"(b));
    asm("v_add_u32 %0, 0x20000, %1" : "=v"(b2) : "v"(b));
  }
  template <int OFF> DEV u32 at() const { return (OFF < 65536 ? b0 : OFF < 131072 ? b1 : b2) + (OFF & 65535); }
};
#define AT(la, off) la.template at<(off)>()
DEV f32x4 mfma(bf16x8 a, bf16x8 b, f32x4 c) { return __builtin_amdgcn_mfma_f32_16x16x32_bf16(a, b, c, 0, 0, 0); }
DEV float blo(u32 v) { return __builtin_bit_cast(float, v << 16); }
DEV float bhi(u32 v) { return __builtin_bit_cast(float, v & 0xffff0000u); }
DEV u32 pk2(float a, float b) { f32x2 x = {a, b}; return __builtin_bit_cast(u32, __builtin_convertvector(x, bf2)); }
DEV float rbf(float a) { return blo(pk2(a, a)); }
#define QP " quad_perm:[1,0,3,2] row_mask:0xf bank_mask:0xf\n\t"
DEV void cmac5(float (&t)[5], const float (&a)[5], const float (&zr)[5], const float (&zs)[5], float b0, float b1, float b2, float b3, float b4) {
  float t0, t1, t2, t3, t4;
  asm("v_fma_f32 %0, %10, %5, %20\n\tv_fma_f32 %1, %11, %6, %21\n\tv_fma_f32 %2, %12, %7, %22\n\tv_fma_f32 %3, %13, %8, %23\n\tv_fma_f32 %4, %14, %9, %24\n\t"
      "v_fmac_f32_dpp %0, %5, %15" QP "v_fmac_f32_dpp %1, %6, %16" QP "v_fmac_f32_dpp %2, %7, %17" QP "v_fmac_f32_dpp %3, %8, %18" QP "v_fmac_f32_dpp %4, %9, %19" QP
      : "=&v"(t0), "=&v"(t1), "=&v"(t2), "=&v"(t3), "=&v"(t4)
      : "v"(a[0]), "v"(a[1]), "v"(a[2]), "v"(a[3]), "v"(a[4]), "v"(zr[0]), "v"(zr[1]), "v"(zr[2]), "v"(zr[3]), "v"(zr[4]),
        "v"(zs[0]), "v"(zs[1]), "v"(zs[2]), "v"(zs[3]), "v"(zs[4]), "v"(b0), "v"(b1), "v"(b2), "v"(b3), "v"(b4));
  t[0] = t0; t[1] = t1; t[2] = t2; t[3] = t3; t[4] = t4;
}
DEV void cmul5(float (&t)[5], const float (&a)[5], const float (&zr)[5], const float (&zs)[5]) {
  float t0, t1, t2, t3, t4;
  asm("v_mul_f32 %0, %10, %5\n\tv_mul_f32 %1, %11, %6\n\tv_mul_f32 %2, %12, %7\n\tv_mul_f32 %3, %13, %8\n\tv_mul_f32 %4, %14, %9\n\t"
      "v_fmac_f32_dpp %0, %5, %15" QP "v_fmac_f32_dpp %1, %6, %16" QP "v_fmac_f32_dpp %2, %7, %17" QP "v_fmac_f32_dpp %3, %8, %18" QP "v_fmac_f32_dpp %4, %9, %19" QP
      : "=&v"(t0), "=&v"(t1), "=&v"(t2), "=&v"(t3), "=&v"(t4)
      : "v"(a[0]), "v"(a[1]), "v"(a[2]), "v"(a[3]), "v"(a[4]), "v"(zr[0]), "v"(zr[1]), "v"(zr[2]), "v"(zr[3]), "v"(zr[4]),
        "v"(zs[0]), "v"(zs[1]), "v"(zs[2]), "v"(zs[3]), "v"(zs[4]));
  t[0] = t0; t[1] = t1; t[2] = t2; t[3] = t3; t[4] = t4;
}
#define B5(x) x[0], x[1], x[2], x[3], x[4]
DEV void rows16(float v, float& ev, float& od) { const u32 u = __builtin_bit_cast(u32, v); auto r = __builtin_amdgcn_permlane16_swap(u, u, false, false);
  const u32 r0 = r[0], r1 = r[1]; ev = __builtin_bit_cast(float, r0); od = __builtin_bit_cast(float, r1); }
DEV void rows32(float v, float& lo, float& hi) { const u32 u = __builtin_bit_cast(u32, v); auto r = __builtin_amdgcn_permlane32_swap(u, u, false, false);
  const u32 r0 = r[0], r1 = r[1]; lo = __builtin_bit_cast(float, r0); hi = __builtin_bit_cast(float, r1); }
DEV float rowsum(float v) { float a, b; rows16(v, a, b); rows32(a + b, a, b); return a + b; }
struct LnG { float nm, k0, k1, ir; };
DEV LnG ln_consts(float mean, float den) { const float r = __builtin_amdgcn_rcpf(den), k0 = -2.3022082f * r; return LnG{-mean, k0, 0.044715f * r * r * k0, den}; }
DEV u32 ln_gelu2(f32x2 acc, f32x2 cs, const LnG& g) {
  const f32x2 a = __builtin_elementwise_fma(f32x2{g.nm, g.nm}, cs, acc);
  const f32x2 u = a * __builtin_elementwise_fma(a * a, f32x2{g.k1, g.k1}, f32x2{g.k0, g.k0});
  const f32x2 d = __builtin_elementwise_fma(f32x2{__builtin_amdgcn_exp2f(u[0]), __builtin_amdgcn_exp2f(u[1])}, f32x2{g.ir, g.ir}, f32x2{g.ir, g.ir});
  const f32x2 y = a * f32x2{__builtin_amdgcn_rcpf(d[0]), __builtin_amdgcn_rcpf(d[1])};
  return pk2(y[0], y[1]);
}
DEV u32x2 ln_gelu4(f32x4 acc, f32x4 cs, const LnG& g) { return u32x2{ln_gelu2(acc.xy, cs.xy, g), ln_gelu2(acc.zw, cs.zw, g)}; }
DEV float ln_den(float var) { return rbf(__builtin_amdgcn_sqrtf(rbf(rbf(var) + 1e-5f))); }
#define PIN(x) asm volatile("" : "+v"(x))
DEV u32x4 ldw(const char* p) { return *(const u32x4*)p; }
DEV f32x4 rbf4(f32x4 v) { const u32 a = pk2(v[0], v[1]), b = pk2(v[2], v[3]); return f32x4{blo(a), bhi(a), blo(b), bhi(b)}; }
DEV f32x4 ln_apply(const u32 (&xp)[2], float mu, float r) { return rbf4(rbf4(f32x4{blo(xp[0]), bhi(xp[0]), blo(xp[1]), bhi(xp[1])} - mu) * r); }
DEV void sincos_cw(float a, float& sn, float& cs) {
  const float k = __builtin_rintf(a * 0.63661977237f);
  float r = fmaf(k, -1.57079601287841796875f, a);
  r = fmaf(k, -3.1391647326017846353e-7f, r);
  r = fmaf(k, -5.3903025299577647655e-15f, r);
  const float z = r * r;
  const float s = fmaf(fmaf(fmaf(-1.9515295891e-4f, z, 8.3321608736e-3f), z, -1.6666654611e-1f), z * r, r);
  const float c = fmaf(fmaf(fmaf(2.443315711809948e-5f, z, -1.388731625493765e-3f), z, 4.166664568298827e-2f), z * z, fmaf(z, -0.5f, 1.f));
  const u32 n = (u32)(int)k;
  const float a0 = (n & 1) ? c : s, a1 = (n & 1) ? s : c;
  sn = __builtin_bit_cast(float, __builtin_bit_cast(u32, a0) ^ ((n & 2) << 30));
  cs = __builtin_bit_cast(float, __builtin_bit_cast(u32, a1) ^ (((n + 1) & 2) << 30));
  if (__builtin_expect(!(fabsf(a) < 8192.f), 0)) sincosf(a, &sn, &cs);
}
#define SB() __builtin_amdgcn_sched_barrier(0)
#define CBAR() do { SB(); asm volatile("" ::: "memory"); __builtin_amdgcn_s_barrier(); asm volatile("" ::: "memory"); SB(); } while (0)
#define LGKM0() do { SB(); __builtin_amdgcn_s_waitcnt(0xC07F); SB(); } while (0)
template <int NM, int NR> DEV void interleave() {
  sfor<0, NM>([&](auto I) {
    constexpr int i = I.value, G = (NR + 1) / 2 < NM - 2 ? (NR + 1) / 2 : NM - 2;
    constexpr int lo = i < G ? (NR * i + G - 1) / G : NR, hi = i < G ? (NR * (i + 1) + G - 1) / G : NR;
    __builtin_amdgcn_sched_group_barrier(0x8, 1, 0);
    if constexpr (hi > lo) __builtin_amdgcn_sched_group_barrier(0x100, hi - lo, 0);
  });
}
#define DRAIN() do { SB(); asm volatile("s_waitcnt vmcnt(0)" ::: "memory"); SB(); } while (0)
#define FLAG(kind) do { if (tid == 0) __hip_atomic_store((u64*)(ws + (kind) + (t * 8 + j) * 128), tag, __ATOMIC_RELAXED, __HIP_MEMORY_SCOPE_AGENT); SB(); } while (0)
DEV u32x4 widen(u32x2 a, u32x2 b) {
  auto r0 = __builtin_amdgcn_permlane16_swap(a[0], b[0], false, false);
  auto r1 = __builtin_amdgcn_permlane16_swap(a[1], b[1], false, false);
  u32x4 o = {r0[0], r1[0], r0[1], r1[1]};
  return o;
}

#define GL "global_load_lds_dwordx4 "
#define M0S "s_mov_b32 m0, %0\n\ts_nop 0\n\t"
#define G1(m, g, a, SC) asm volatile(M0S GL "%2, %1" SC ::"s"(m), "s"(g), "v"(a) : "memory")
#define G2(m, g, a, b, SC) asm volatile(M0S GL "%2, %1" SC "\n\t" GL "%3, %1 offset:1024" SC ::"s"(m), "s"(g), "v"(a), "v"(b) : "memory")
#define G4(m, g, a, b, c, d, SC) asm volatile(M0S GL "%2, %1" SC "\n\t" GL "%3, %1 offset:1024" SC "\n\t" GL "%4, %1 offset:2048" SC "\n\t" GL "%5, %1 offset:3072" SC ::"s"(m), "s"(g), "v"(a), "v"(b), "v"(c), "v"(d) : "memory")
#define G3H(m, g, a, b, c, SC) asm volatile(M0S GL "%2, %1" SC "\n\t" GL "%3, %1 offset:1024" SC "\n\ts_mov_b32 exec_hi, 0\n\t" GL "%4, %1 offset:2048" SC "\n\ts_mov_b32 exec_hi, -1" ::"s"(m), "s"(g), "v"(a), "v"(b), "v"(c) : "memory")
#define G2W(m, g, a, b) asm volatile(M0S GL "%2, %1\n\t" GL "%3, %1 offset:2048" ::"s"(m), "s"(g), "v"(a), "v"(b) : "memory")
#define LWAIT(n) asm volatile("s_waitcnt vmcnt(" #n ")" ::: "memory")
#define LBAR() do { asm volatile("" ::: "memory"); __builtin_amdgcn_s_barrier(); asm volatile("" ::: "memory"); } while (0)
#define L_X(s, o)   G2(lb + (o) + lw * 2048u, gx + RK1(s) * 128, vx0, vx1, "")
#define L_W1(s, o)  G3H(lb + (o) + lw * 2560u, gw1 + RK1(s) * 81920, vw0, vw1, vw2, "")
#define RK(s, U, n) ((U) && (s) < ((n) / (2 * (U) + !(U))) * 2 * (U) ? ((s) ^ (grp * (U))) : (u32)(s))
#define RK1(s) RK(s, SW1, 10u)
#define RK1I(k) RK(k, SW1, 10u)
#define L_W2(s, o)  G3H(lb + (o) + lw * 2560u, gw2 + RK(s, SW3, 10u) * 10240, vl, vl, vl, " sc1")
#define L_W4(s, o)  G3H(lb + (o) + lw * 2560u, gw4 + RK(s, SW6, 24u) * 10240, vl, vl, vl, " sc1")
#define L_HD(s, o)  G2(lb + (o) + lw * 2048u, ghd + RK(s, SW6, 24u) * 8192, vl, vl, " sc1")
#define L_BM(s, o)  do { G2W(lb + (o) + mbm, gbm + RK1(s) * 128, vb0, vb1); G2W(lb + (o) + mbm + 4096u, gbm + RK1(s) * 128, vb2, vb3); \
                         G1(lb + (o) + mbm + 8192u, gbm + RK1(s) * 128, vb4, ""); } while (0)
#define L_H1(s, o)  G2(lb + (o) + lw * 2048u, gh1 + RK(s, SW3, 10u) * 8192, vl, vl, " sc1")
#define L_X1(s, o)  G2(lb + (o) + lw * 2048u, gx1 + RK(s, SW4, 10u) * 8192, vl, vl, " sc1")
#define L_HH(s, o)  G2(lb + (o) + lw * 2048u, ghh + RK(s, SW5, 20u) * 8192, vl, vl, " sc1")
#define GB(m, a) asm volatile(M0S "buffer_load_dwordx4 %2, %1, 0 offen sc1 lds" ::"s"(m), "s"(ragg), "v"(a) : "memory")
#define L_AGG(o)    do { GB(lb + (o) + lw * 5120u, va0); GB(lb + (o) + lw * 5120u + 1024u, va1); GB(lb + (o) + lw * 5120u + 2048u, va2); \
                         GB(lb + (o) + lw * 5120u + 3072u, va3); GB(lb + (o) + lw * 5120u + 4096u, va4); } while (0)
#define L_ST(o)     G1(lb + (o) + lw * 1024u, gst, vl, " sc1")
#define L_W3(s, o)  do { G4(lb + (o) + lw * 6144u, gw3 + RK(s, SW4, 10u) * 196608, v30, v31, v32, v33, ""); G2(lb + (o) + lw * 6144u + 4096u, gw3 + RK(s, SW4, 10u) * 196608, v34, v35, ""); } while (0)
#define G3(m, g, a, b, c) asm volatile(M0S GL "%2, %1\n\t" GL "%3, %1 offset:1024\n\t" GL "%4, %1 offset:2048" ::"s"(m), "s"(g), "v"(a), "v"(b), "v"(c) : "memory")
#define L_W3A(s, o) G3(lb + (o) + lw * 6144u, gw3 + RK(s, SW4, 10u) * 196608, v30, v31, v32)
#define L_W3B(s, o) do { asm volatile(M0S GL "%2, %1 offset:3072" ::"s"(lb + (o) + lw * 6144u), "s"(gw3 + RK(s, SW4, 10u) * 196608), "v"(v33) : "memory"); \
                         G2(lb + (o) + lw * 6144u + 4096u, gw3 + RK(s, SW4, 10u) * 196608, v34, v35, ""); } while (0)
#define GV "global_load_dwordx4 "
#define DW "ds_write_b128 %0, "
#define RG3(g, a, b, c, o0, o1, o2, q0, q1, q2, SC) asm volatile(GV q0 ", %1, %0 offset:" #o0 SC "\n\t" GV q1 ", %2, %0 offset:" #o1 SC "\n\t" \
    GV q2 ", %3, %0 offset:" #o2 SC ::"s"(g), "v"(a), "v"(b), "v"(c) : "memory")
#define R_W3A(s, q0, q1, q2) RG3(gw3 + RK(s, SW4, 10u) * 196608, v30, v31, v32, 0, 1024, 2048, q0, q1, q2, "")
#define R_W3B(s, q0, q1, q2) RG3(gw3 + RK(s, SW4, 10u) * 196608, v33, v34, v35, 3072, 0, 1024, q0, q1, q2, "")
#define W_W3(o, q0, q1, q2, q3, q4, q5) asm volatile(DW q0 "\n\t" DW q1 " offset:1024\n\t" DW q2 " offset:2048\n\t" DW q3 " offset:3072\n\t" \
    DW q4 " offset:4096\n\t" DW q5 " offset:5120" ::"v"(lb + (o) + lw * 6144u + vl) : "memory")
#define LGW() asm volatile("s_waitcnt lgkmcnt(0)" ::: "memory")
#define L_C(s, o)   G3H(lb + (o) + lw * 2560u, (RK(s, SW5, 20u) < 10 ? gcr : gci - 1280) + RK(s, SW5, 20u) * 128, vc0, vc1, vc2, "")
#define POLL_LD(off) asm volatile("global_load_dwordx2 v[250:251], %0, %1 sc1" ::"v"(off), "s"(ws) : "memory", "v250", "v251")
#define POLL_GET(n, r0, r1) asm volatile("s_waitcnt vmcnt(" #n ")\n\tv_mov_b32 %0, " r0 "\n\tv_mov_b32 %1, " r1 : "=v"(pv0), "=v"(pv1)::"memory")
#define POLL_OK(ign) __all((ign) || (pv0 == tlo && pv1 == thi))
#define POLL_SPIN(off, ign, id) do { \
    if (lw == 0) { \
      _Pragma("unroll 1") for (u32 spin = 0; spin < SPIN_CAP; ++spin) { \
        POLL_LD(off); POLL_GET(0, "v250", "v251"); \
        if (POLL_OK(ign)) break; __builtin_amdgcn_s_sleep(2); } \
      *(AS3 volatile u32*)(lb + L_SYNC) = (id); \
    } else { \
      _Pragma("unroll 1") for (u32 spin = 0; spin < 64 * SPIN_CAP; ++spin) { \
        if (*(AS3 volatile u32*)(lb + L_SYNC) >= (id)) break; __builtin_amdgcn_s_sleep(1); } } \
    asm volatile("" ::: "memory"); } while (0)
#define POLL_CHK(n, off, ign, id) do { POLL_GET(n, "v250", "v251"); POLL_FIN(off, ign, id); } while (0)
#define POLL_FIN(off, ign, id) do { \
    if (lw == 0 && POLL_OK(ign)) *(AS3 volatile u32*)(lb + L_SYNC) = (id); else POLL_SPIN(off, ign, id); } while (0)
#define POLL_ISSUE_RT() POLL_LD(pf_s + W_FRT)
#define POLL_ISSUE_H1() POLL_LD(pf_h1)
#define POLL_ISSUE_AGG() POLL_LD(pf_s + W_FAGG)
#define POLL_ISSUE_HH() POLL_LD(pf_t + W_FHH)
#define POLL_ISSUE_HD() POLL_LD(pf_hd)
#define JOB_TAG() TAG_READY()
#define POLL_CHECK_RTB(n) do { if (lw == 0) POLL_CHK(n, pf_s + W_FRT, false, 1u); } while (0)
#define POLL_CHECK_H1B(n) do { if (lw == 0) POLL_CHK(n, pf_h1, false, 2u); } while (0)
#define POLL_CHECK_HHB(n) do { if (lw == 0) POLL_CHECK_HH(n); } while (0)
#define POLL_CHECK_AGGB(n) do { if (lw == 0) POLL_CHK(n, pf_s + W_FAGG, (lane & 31) >= t, 3u); } while (0)
#define POLL_SPIN_X1() do { \
    if (lw == 0) { \
      _Pragma("unroll 1") for (u32 spin = 0; spin < SPIN_CAP; ++spin) {     \
        POLL_LD(pf_t + W_FX1); __builtin_amdgcn_s_sleep(5); \
        asm volatile("global_load_dwordx2 v[252:253], %0, %1 sc1" ::"v"(pf_t + W_FX1), "s"(ws) : "memory", "v252", "v253"); \
        POLL_GET(1, "v250", "v251"); if (POLL_OK(false)) break; \
        POLL_GET(0, "v252", "v253"); if (POLL_OK(false)) break; } \
      *(AS3 volatile u32*)(lb + L_SYNC) = 4u; \
    } else POLL_SPIN(pf_t + W_FX1, false, 4u); \
    } while (0)
#define POLL_CHECK_HH(n) do { POLL_CHK(n, pf_t + W_FHH, false, 5u); \
      \
    if (t == 31 && j == 0 && lw == 0 && lane == 0) \
      __hip_atomic_store((u64*)(ws + W_EPOCH), ((u64)thi << 32) | tlo, __ATOMIC_RELAXED, __HIP_MEMORY_SCOPE_AGENT); } while (0)
#define POLL_CHECK_HDB(n) do { if (lw == 0) POLL_CHK(n, pf_hd, false, 6u); } while (0)
#define TOUCH(a) asm volatile(M0S "global_load_lds_dword %1, off" ::"s"(lb + L_DUMMY), "v"(a) : "memory")
#define PREFETCH_1() TOUCH(pfa1)
#define PREFETCH_2() TOUCH(pfa2)
#define JOB_ST() do { if (lw == 3) { float sm = 0.f; \
    sfor<0, 8>([&](auto I) { sp[I] = ldf2(lb + L_STO + I * 512 + lane * 8); sm += sp[I][0]; }); \
    mn = sm * (1.f / 640.f); PIN(mn); } } while (0)
#define JOB_SU() do { if (lw == 3) { m2 = 0.f; \
    sfor<0, 8>([&](auto I) { const float d = fmaf(sp[I][0], 1.f / 80.f, -mn); m2 += fmaf(80.f * d, d, sp[I][1]); }); PIN(m2); } } while (0)
#define JOB_SV() do { if (lw == 3) { const LnG g_ = ln_consts(rbf(mn), ln_den(m2 * (1.f / 640.f))); \
    *(AS3 f32x4*)(lb + L_ST2 + lane * 16) = f32x4{g_.nm, g_.k0, g_.k1, g_.ir}; LGW(); } } while (0)
#define TAG_READY() do { POLL_GET(63, "v248", "v249"); u64 e_ = (((u64)pv1 << 32) | pv0) + 1; tlo = (u32)e_; thi = (u32)(e_ >> 32); } while (0)

__global__ __launch_bounds__(512, 2) void s5_mixer_kernel(
    const u16* __restrict__ x, const u16* __restrict__ lre, const u16* __restrict__ lim, const u16* __restrict__ Bre,
    const u16* __restrict__ Bim, const u16* __restrict__ Cre, const u16* __restrict__ Cim, const u16* __restrict__ ldt,
    const u16* __restrict__ dsk, const u16* __restrict__ w1, const u16* __restrict__ w2, const u16* __restrict__ w3,
    const u16* __restrict__ w4, float* __restrict__ out, char* ws) {
  __shared__ __attribute__((aligned(1024))) char lds[163840];
  const u32 lb = (u32)(size_t)(AS3 char*)lds;
  u64 pc0; asm volatile("s_getpc_b64 %0\n.Ls5_code_begin:" : "=s"(pc0));
  const int tid = threadIdx.x, lane = tid & 63;
  const int wid = __builtin_amdgcn_readfirstlane(tid >> 6);
  const int xcd = blockIdx.x & 7, bi = blockIdx.x >> 3;
  const int t = (xcd >> 1) * 8 + (bi >> 2), j = (xcd & 1) * 4 + (bi & 3);

  const u32 grp = (t + j) & 1;
  if (wid < 4) {
  const int w = wid, c = lane & 15, q = lane >> 4, m = w & 1, nh = w >> 1;
  if (wid == 3) {
    u32 len; asm volatile("s_mov_b32 %0, .Ls5_code_end-.Ls5_code_begin" : "=s"(len));
    const u32 nl = len >> 7, per = (nl + 31) >> 5, ln = bi * per + (lane < per ? lane : per - 1);
    TOUCH((const char*)pc0 + (size_t)((ln < nl ? ln : nl - 1) << 7));
  }
  u64 tag = __hip_atomic_load((const u64*)(ws + W_EPOCH), __ATOMIC_RELAXED, __HIP_MEMORY_SCOPE_AGENT);
  const auto rs = __builtin_amdgcn_make_buffer_rsrc(ws, 0, W_END, 0x00020000);
  const int pch = w * 20 + (lane < 20 ? lane : 19);
  typedef __attribute__((address_space(1))) const u16* gp16;
  gp16 q0_ = (gp16)ldt, q1_ = (gp16)lre, q2_ = (gp16)lim, q3_ = (gp16)dsk;
  asm volatile("" : "+s"(q0_), "+s"(q1_), "+s"(q2_), "+s"(q3_));
  u32 pl0 = q0_[j * 80 + pch], pl1 = q1_[j * 80 + pch], pl2 = q2_[j * 80 + pch], pl3 = q3_[j * 80 + pch];
  asm volatile("" ::"s"(w2), "s"(w4), "s"(out));
  u32x4 w2c, w4a, w4b;
  auto roff = [&](u32 n) { const u32 pos = n / 10, ch = n % 10, k = (pos & ~12u) | ((pos & 4) << 1) | ((pos & 8) >> 1); return k * 1280 + j * 160 + ch * 16; };
  u32 ro2 = roff(t * 200 + (tid < 200 ? tid : 199)), ro4a = roff(t * 480 + tid), ro4b = roff(t * 480 + 256 + (tid < 224 ? tid : 223));
  PIN(ro2);
  { const char* w2p = (const char*)w2; asm volatile("" : "+s"(w2p)); w2c = *(__attribute__((address_space(1))) const u32x4*)(w2p + ro2); }
  PIN(ro4a); PIN(ro4b);
  const bf16x8 ones = {0x3F80, 0x3F80, 0x3F80, 0x3F80, 0x3F80, 0x3F80, 0x3F80, 0x3F80};
  const f32x4 zero4 = {0.f, 0.f, 0.f, 0.f};
  const int ta = c >> 2, tp = c & 3;
  const u32 wb = lb + (ta + 4 * (q & 1) + 16 * (q >> 1)) * 160 + tp * 8;
  const u32 ftr = ((c >> 3) & 1) | ((q & 1) << 1);
  const u32 hb = lb + (8 * q + ta) * 128 + ((w ^ ftr) << 5) + tp * 8;
  const u32 blk = lb + (q >> 1) * 2048 + c * 32 + (q & 1) * 16;
  const int row = 16 * w + c;
  const u32 fx2 = ((c >> 1) & 1) | ((c >> 2) & 2) | (((c >> 2) ^ w) & 1) << 2;
  float mu, rs1;
  u32 xb[5][2];
  u32 csw = lb + L_CS + w * 320, csr = lb + L_CS + (w ^ 1) * 320;

  {
    const u32 xa_ = (lb + row * 128 + ((q ^ fx2) << 4)) ^ (m << 6);
    const LA xa0(xa_), xa1(xa_ ^ 64), wa0(wb + m * 5120), wa1(wb + (1 - m) * 5120);
    f32x4 acc[5], cs[5];
    sfor<0, 5>([&](auto N) { acc[N] = zero4; cs[N] = zero4; });
    f32x4 sa = zero4, qa = zero4;
    u32 xo[5], cw4 = csw + q * 16, cr4 = csr + q * 16, sto = lb + L_STATS + row * 4;
    sfor<0, 5>([&](auto N) { const int n = j * 80 + N * 16 + q * 4;
      const u32 sl = RK1I((u32)(n >> 6));
      xo[N] = lb + sl * 8192 + row * 128 + ((((n & 63) >> 3) ^ fx2) << 4) + (q & 1) * 8; PIN(xo[N]); });
    const int g4 = j >> 1, sh = (g4 & 1) ? (g4 >> 1) + 1 : -(g4 >> 1) - 1, pos = t * 64 + row - sh;
    const u32 hbase = W_H1 + (pos >> 6) * 81920 + j * 10240 + (pos & 63) * 32;
    u32 o16 = (u32)pos < 2048u ? hbase + (q & 1) * 2048 + (q >> 1) * 16 : 0xFFFF0000u, o8 = (u32)pos < 2048u ? hbase + 8192 + q * 8 : 0xFFFF0000u;
    f32x4 dg = {q == ta && tp == 0 ? 1.f : 0.f, q == ta && tp == 1 ? 1.f : 0.f, q == ta && tp == 2 ? 1.f : 0.f, q == ta && tp == 3 ? 1.f : 0.f};
    PIN(cw4); PIN(cr4); PIN(sto); PIN(o16); PIN(o8); PIN(dg);

    bf16x8 fx[2][2], fw[2][2][5];
    auto mm = [&](auto P) {
      sfor<0, 2>([&](auto U) {
        sfor<0, 5>([&](auto N) { acc[N] = mfma(fw[P][U][N], fx[P][U], acc[N]); });
        if constexpr (U.value == 0) sfor<0, 5>([&](auto N) { cs[N] = mfma(fw[P][0][N], ones, cs[N]); });
        sa = mfma(ones, fx[P][U], sa); qa = mfma(fx[P][U], fx[P][U], qa);
      });
    };
    sfor<0, 10>([&](auto S) {
      constexpr int P = S & 1;
      CBAR();
      if constexpr (S == 9) { w4a = ldw((const char*)w4 + ro4a); w4b = ldw((const char*)w4 + ro4b); SB(); }
      fx[P][0] = ld128(AT(xa0, ASLOT[S]));
      sfor<0, 5>([&](auto N) { fw[P][0][N] = ldtr(AT(wa0, BSLOT[S] + N * 32), AT(wa0, BSLOT[S] + N * 32 + 1280)); });
      fx[P][1] = ld128(AT(xa1, ASLOT[S]));
      sfor<0, 5>([&](auto N) { fw[P][1][N] = ldtr(AT(wa1, BSLOT[S] + N * 32), AT(wa1, BSLOT[S] + N * 32 + 1280)); });
      if constexpr (S > 0) { mm(std::integral_constant<int, P ^ 1>{}); interleave<19, 22>(); }
      if constexpr (S == 0) { SB(); asm volatile("" : "+v"(pl0), "+v"(pl1), "+v"(pl2), "+v"(pl3), "+v"(tag)); tag += 1; }
      if constexpr (S == 0) if (lane < 20) {
        #define CSQ() do { float r_ = zr * zr - zi * zi; zi = 2.f * zr * zi; zr = r_; } while (0)
        const float a = blo(pl1), b = blo(pl2), dt = rbf(__builtin_amdgcn_exp2f(blo(pl0) * 1.4426950409f));
        const float e = __builtin_amdgcn_exp2f(dt * a * 1.4426950409f);
        float zr, zi; sincos_cw(dt * b, zi, zr); zr *= e; zi *= e;
        const float nr = zr - 1.f, den = __builtin_amdgcn_rcpf(a * a + b * b);
        AS3 f32x2* tab = (AS3 f32x2*)(lb + L_TAB + pch * 8);
        *(AS3 float*)(lb + L_DSK + pch * 4) = blo(pl3);
        tab[0] = f32x2{zr, zi}; tab[80] = f32x2{(nr * a + zi * b) * den, (zi * a - nr * b) * den};
        CSQ(); CSQ(); CSQ(); const float r8_ = zr, i8_ = zi; tab[160] = f32x2{zr, zi};
        CSQ(); tab[240] = f32x2{zr, zi}; tab[320] = f32x2{zr * r8_ - zi * i8_, zr * i8_ + zi * r8_};
        CSQ(); tab[400] = f32x2{zr, zi};
        CSQ(); tab[480] = f32x2{zr, zi};
        CSQ(); CSQ(); CSQ(); const float rL = zr, iL = zi; tab[560] = f32x2{zr, zi};
        CSQ(); tab[640] = f32x2{zr, zi}; tab[720] = f32x2{zr * rL - zi * iL, zr * iL + zi * rL};
      }
      if constexpr (S == 2) {
        SB();
        __builtin_amdgcn_raw_buffer_store_b128(w2c, rs, tid < 200 ? W_W2R + j * 102400 + (t * 200 + tid) * 16 : 0xFFFF0000u, 0, 16);
      }
      if constexpr (S == 3) if ((t == 0 && sh < 0 && w == 0) || (t == 31 && sh > 0 && w == 3)) {
        const int z = (lane >> 1) & 1, ash = sh < 0 ? -sh : sh, zp = sh < 0 ? z : 63 - z;
        const u32 zo = (lane < 20 && z < ash) ? W_H1 + t * 81920 + j * 10240 + (lane >> 2) * 2048 + zp * 32 + (lane & 1) * 16 : 0xFFFF0000u;
        __builtin_amdgcn_raw_buffer_store_b128(u32x4{0, 0, 0, 0}, rs, zo, 0, 16);
      }
      if constexpr (S == 9) sfor<0, 5>([&](auto N) { const u32x2 xv = ldu2(xo[N]); xb[N][0] = xv[0]; xb[N][1] = xv[1]; });
      if constexpr (S == 4) DRAIN();
      if constexpr (S == 5) FLAG(W_FRT);
      LGKM0();
    });
    CBAR();
    sfor<0, 2>([&](auto U) { qa = mfma(fx[1][U], fx[1][U], qa); sa = mfma(ones, fx[1][U], sa); });
    sfor<0, 5>([&](auto N) { cs[N] = mfma(fw[1][0][N], ones, cs[N]); });
    SB();
    sfor<0, 2>([&](auto U) { sfor<0, 5>([&](auto N) { acc[N] = mfma(fw[1][U][N], fx[1][U], acc[N]); }); });
    const f32x2 dq = __builtin_elementwise_fma(qa.xy, dg.xy, qa.zw * dg.zw);
    const float s2 = rowsum(dq[0] + dq[1]);
    const float mean = sa[0] * (1.f / 640.f), var = fmaxf(s2 * (1.f / 640.f) - mean * mean, 0.f), den = ln_den(var);
    mu = rbf(mean);
    rs1 = __builtin_amdgcn_rcpf(den);
    sfor<0, 10>([&](auto) { __builtin_amdgcn_sched_group_barrier(0x8, 1, 0); __builtin_amdgcn_sched_group_barrier(0x402, 2, 0); });
    SB();
    if (c == 0) sfor<0, 5>([&](auto N) { *(AS3 f32x4*)(cw4 + N * 64) = cs[N]; });
    if (q == 0) { *(AS3 float*)sto = -mu; *(AS3 float*)(sto + 256) = rs1; }
    const LnG lg = ln_consts(fmaf(rbf((mean - mu) * rs1), den, mu), den * ln_den(var * rs1 * rs1));
    LGKM0();
    CBAR();
    u32x2 hp[5];
    sfor<0, 5>([&](auto N) { hp[N] = ln_gelu4(acc[N], cs[N] + ldf4(cr4 + N * 64), lg); });
    __builtin_amdgcn_raw_buffer_store_b128(widen(hp[0], hp[1]), rs, o16, 0, 16);
    __builtin_amdgcn_raw_buffer_store_b128(widen(hp[2], hp[3]), rs, o16 + 4096, 0, 16);
    __builtin_amdgcn_raw_buffer_store_b64(hp[4], rs, o8, 0, 16);
  }

  float bu[8][5], inc[5], lr[5], ls[5], ev[5], lo[5];
  f32x4 xn[5];
  const float sgn = (c & 1) ? 1.f : -1.f;
  u32 tot;
  u32 tb0 = lb + L_TAB + (nh * 40 + (c >> 1)) * 8;
  asm("" : "+v"(tb0));
  {
    const u32 xr = lb + (32 * m + 8 * ta + tp) * 128 + ((q ^ (c >> 1)) << 4);
    const LA xp0(xr ^ (m << 6)), xp1(xr ^ (m << 6) ^ 64);
    const u32 f3 = (c & 1) | ((c >> 1) & 6);
    const u32 bp_ = (lb + (nh * 80 + (c & 1) * 8 + (c >> 1)) * 128 + ((q ^ f3) << 4)) ^ (m << 6);
    const LA bp0(bp_), bp1(bp_ ^ 64);
    f32x4 acc[2][5], cs[5];
    sfor<0, 5>([&](auto N) { acc[0][N] = zero4; acc[1][N] = zero4; cs[N] = zero4; });
    bf16x8 fa[2][2], fb[2][5];
    u32 cw1, cr1, sta, wo4;
    auto rd = [&](auto S, auto U) {
      fa[U][0] = ld128(AT((U ? xp1 : xp0), ASLOT[S])); fa[U][1] = ld128(AT((U ? xp0 : xp1), ASLOT[S] + 512));
      sfor<0, 5>([&](auto N) { fb[U][N] = ld128(AT((U ? bp1 : bp0), BSLOT[S] + N * 2048)); });
    };
    auto mm = [&](auto U) {
      sfor<0, 5>([&](auto N) {
        acc[0][N] = mfma(fa[U][0], fb[U][N], acc[0][N]); acc[1][N] = mfma(fa[U][1], fb[U][N], acc[1][N]);
        if constexpr (U.value == 0) cs[N] = mfma(ones, fb[0][N], cs[N]);
      });
    };
    constexpr std::integral_constant<int, 0> U0{}; constexpr std::integral_constant<int, 1> U1{};
    sfor<10, 20>([&](auto S) {
      CBAR();
      if constexpr (S == 13) FLAG(W_FH1);
      rd(S, U0);
      if constexpr (S > 10) { mm(U1); interleave<10, 7>(); }
      SB();
      rd(S, U1); mm(U0); interleave<15, 7>();
      if constexpr (S == 12) DRAIN();
      if constexpr (S >= 14 && S < 19) {
        constexpr int N = S - 14;
        xn[N] = ln_apply(xb[N], mu, rs1); PIN(xn[N]);
      }
      if constexpr (S == 19) { cw1 = csw + c * 4; cr1 = csr + c * 4; sta = lb + L_STATS + (32 * m + 8 * q) * 4; tot = lb + L_TOT + nh * 320 + c * 4;
        wo4 = W_W4R + j * 245760 + (t * 480 + tid) * 16; PIN(cw1); PIN(cr1); PIN(sta); PIN(tot); PIN(wo4); }
      LGKM0();
    });
    if (q == 0) sfor<0, 5>([&](auto N) { *(AS3 float*)(cw1 + N * 64) = cs[N][0]; });
    CBAR();
    mm(U1);
    LGKM0();
    CBAR();
    __builtin_amdgcn_raw_buffer_store_b128(w4a, rs, wo4, 0, 16);
    __builtin_amdgcn_raw_buffer_store_b128(w4b, rs, tid < 224 ? wo4 + 4096 : 0xFFFF0000u, 0, 16);
    const f32x4 nm[2] = {ldf4(sta), ldf4(sta + 16)}, rr[2] = {ldf4(sta + 256), ldf4(sta + 272)};
    float cc[5], l8r[5], l8s[5], l16r[5], l16s[5];
    sfor<0, 5>([&](auto N) {
      cc[N] = ldf1(cr1 + N * 64) + cs[N][0];
      const f32x2 lam = ldf2(tb0 + N * 64), l8 = ldf2(tb0 + 1280 + N * 64), l16 = ldf2(tb0 + 1920 + N * 64);
      lr[N] = lam[0]; ls[N] = lam[1] * sgn; l8r[N] = l8[0]; l8s[N] = l8[1] * sgn; l16r[N] = l16[0]; l16s[N] = l16[1] * sgn;
    });
    sfor<0, 4>([&](auto I) {
      sfor<0, 5>([&](auto N) {
        const f32x2 v = ((I & 1) ? rr[I >> 1].zw : rr[I >> 1].xy) *
            __builtin_elementwise_fma((I & 1) ? nm[I >> 1].zw : nm[I >> 1].xy, f32x2{cc[N], cc[N]}, (I & 1) ? acc[I >> 1][N].zw : acc[I >> 1][N].xy);
        bu[2 * I][N] = v[0]; bu[2 * I + 1][N] = v[1];
      });
    });
    float a[5], od[5], hi[5];
    sfor<0, 5>([&](auto N) { a[N] = bu[0][N]; });
    sfor<1, 8>([&](auto J) { cmac5(a, a, lr, ls, B5(bu[J])); });
    sfor<0, 5>([&](auto N) { rows16(a[N], ev[N], od[N]); });
    cmac5(a, ev, l8r, l8s, B5(od));
    sfor<0, 5>([&](auto N) { rows32(a[N], lo[N], hi[N]); });
    cmac5(inc, lo, l16r, l16s, B5(hi));
    if (m == 0 && q == 0) sfor<0, 5>([&](auto N) { *(AS3 float*)(tot + N * 64) = inc[N]; });
    LGKM0();
  }

  u32 x1b[5][2];
  u32x4 hq[5];
  u32 ho0, ho1;
  u32 w3a[3];
  {
    const LA ha(blk + w * 512), wl(wb);
    f32x4 acc[5];
    float t0[5], pr[5], ps[5], cr[5], ci[5];
    f32x2 ta_[5];
    u32 o16, ost;
    sfor<0, 5>([&](auto N) { acc[N] = xn[N]; });
    bf16x8 fx[2][2], fw[2][2][5];
    auto mm = [&](auto P) { sfor<0, 2>([&](auto U) { sfor<0, 5>([&](auto N) { acc[N] = mfma(fw[P][U][N], fx[P][U], acc[N]); }); }); };
    sfor<20, 30>([&](auto S) {
      constexpr int P = S & 1;
      CBAR();
      if constexpr (S == 23) FLAG(W_FAGG);
      if constexpr (S == 21) sfor<0, 5>([&](auto N) { ta_[N] = ldf2(tb0 + 640 + N * 64); });
      sfor<0, 2>([&](auto U) {
        fx[P][U] = ld128(AT(ha, ASLOT[S] + U * 4096));
        sfor<0, 5>([&](auto N) { fw[P][U][N] = ldtr(AT(wl, BSLOT[S] + U * 5120 + N * 32), AT(wl, BSLOT[S] + U * 5120 + N * 32 + 1280)); });
      });
      if constexpr (S > 20) { mm(std::integral_constant<int, P ^ 1>{}); interleave<10, S == 21 ? 25 : 22>(); }
      if constexpr (S == 20) {
        SB();
        float l32r[5], l32s[5];
        sfor<0, 5>([&](auto N) { t0[N] = ldf1(tot + N * 64); const f32x2 l32 = ldf2(tb0 + 3200 + N * 64); l32r[N] = l32[0]; l32s[N] = l32[1] * sgn; });
        if (m == 1) {
          cmac5(inc, t0, l32r, l32s, B5(inc));
          float v = inc[0]; v = q == 1 ? inc[1] : v; PIN(v); v = q == 2 ? inc[2] : v; PIN(v); v = q == 3 ? inc[3] : v;
          const u32 ao = W_AGG + (j * 32 + t) * 640 + nh * 320 + c * 4;
          __builtin_amdgcn_raw_buffer_store_b32(__builtin_bit_cast(u32, v), rs, ao + q * 64, 0, 16);
          __builtin_amdgcn_raw_buffer_store_b32(__builtin_bit_cast(u32, inc[4]), rs, q == 0 ? ao + 256 : 0xFFFF0000u, 0, 16);
        }
      }
      if constexpr (S == 22) sfor<0, 5>([&](auto N) { cr[N] = ta_[N][0] * (-2.f * sgn); ci[N] = ta_[N][1] * -2.f; ls[N] = -ls[N]; });
      if constexpr (S >= 23) { SB(); cmul5(bu[S - 23], bu[S - 23], cr, ci); }
      if constexpr (S == 29) cmul5(bu[7], bu[7], cr, ci);
      if constexpr (S == 26) {
        const u32 kap = (c & 1) * 640 + j * 80 + nh * 40 + (c >> 1), f = ((c >> 2) & 1) | ((kap >> 2) & 2);
        ho0 = W_HH + t * 163840 + kap * 128 + (((2 * m + (q >> 1)) ^ f) << 5) + (q & 1) * 16; ho1 = (ho0 ^ 64) + 1024; PIN(ho0); PIN(ho1);
      }
      if constexpr (S == 25) { o16 = W_X1 + t * 81920 + j * 10240 + row * 32 + (q & 1) * 2048 + (q >> 1) * 16;
        ost = q == 0 ? W_ST + (t * 8 + j) * 512 + row * 8 : 0xFFFF0000u; PIN(o16); PIN(ost); }
      if constexpr (S == 22) DRAIN();
      LGKM0();
    });
    CBAR();
    mm(std::integral_constant<int, 1>{});
    sfor<0, 5>([&](auto N) { x1b[N][0] = pk2(acc[N][0], acc[N][1]); x1b[N][1] = pk2(acc[N][2], acc[N][3]); });
    __builtin_amdgcn_raw_buffer_store_b128(widen(u32x2{x1b[0][0], x1b[0][1]}, u32x2{x1b[1][0], x1b[1][1]}), rs, o16, 0, 16);
    __builtin_amdgcn_raw_buffer_store_b128(widen(u32x2{x1b[2][0], x1b[2][1]}, u32x2{x1b[3][0], x1b[3][1]}), rs, o16 + 4096, 0, 16);
    __builtin_amdgcn_raw_buffer_store_b64(u32x2{x1b[4][0], x1b[4][1]}, rs, o16 - (q & 1) * 2048 - (q >> 1) * 16 + 8192 + q * 8, 0, 16);
    f32x2 s1 = {0.f, 0.f}, sq = {0.f, 0.f};
    sfor<0, 5>([&](auto N) { s1 += acc[N].xy + acc[N].zw; sq = __builtin_elementwise_fma(acc[N].xy, acc[N].xy, sq); sq = __builtin_elementwise_fma(acc[N].zw, acc[N].zw, sq); });
    const float sm = rowsum(s1[0] + s1[1]), m2 = fmaxf(fmaf(sm * (-1.f / 80.f), sm, rowsum(sq[0] + sq[1])), 0.f);
    __builtin_amdgcn_raw_buffer_store_b64(u32x2{__builtin_bit_cast(u32, sm), __builtin_bit_cast(u32, m2)}, rs, ost, 0, 16);
    DRAIN();
    CBAR();
    FLAG(W_FX1);
    float car[5], l32r[5], l32s[5];
    {
      float Lr[5], Ls[5], g[8][5];
      u32 ag = lb + L_AGGO + q * 5120 + nh * 320 + c * 4;
      sfor<0, 8>([&](auto U) { sfor<0, 5>([&](auto N) { g[7 - U][N] = ldf1(ag + (7 - U) * 640 + N * 64); }); });
      sfor<0, 5>([&](auto N) { const f32x2 L = ldf2(tb0 + 3840 + N * 64); Lr[N] = L[0]; Ls[N] = L[1] * sgn; car[N] = g[7][N]; });
      f32x2 Lq[5];
      u32 tb1 = tb0; PIN(tb1);
      sfor<0, 5>([&](auto N) { Lq[N] = ldf2(tb0 + (q ? 6 + q : 7) * 640 + N * 64); const f32x2 l32 = ldf2(tb1 + 3200 + N * 64); l32r[N] = l32[0]; l32s[N] = l32[1] * sgn; });
      sfor<1, 8>([&](auto U) { cmac5(car, car, Lr, Ls, B5(g[7 - U])); });
      sfor<0, 5>([&](auto N) { Lr[N] = q ? Lq[N][0] : 1.f; Ls[N] = q ? Lq[N][1] * sgn : 0.f; });
      cmul5(car, car, Lr, Ls);
      sfor<0, 5>([&](auto N) { car[N] = rowsum(car[N]); });
    }
    auto w3f = [&](u32 vv) { return lb + (8 * q + ta) * 384 + (((vv & ~3u) | ((vv ^ ftr) & 3)) << 5) + tp * 8; };
    const LA w3b[3] = {LA(w3f(3 * w)), LA(w3f(3 * w + 1)), LA(w3f(3 * w + 2))};
    sfor<0, 3>([&](auto V) { w3a[V] = w3b[V].b0; });
    float o[8][5];
    {
      float mr[5], ms[5];
      sfor<0, 5>([&](auto N) {
        const f32x2 lq = ldf2(tb0 + (q ? 1 + q : 2) * 640 + N * 64), l8 = ldf2(tb0 + 1280 + N * 64);
        pr[N] = q ? lq[0] : 1.f; ps[N] = q ? lq[1] * sgn : 0.f;
        mr[N] = q == 3 ? l8[0] : (q == 2 ? 1.f : 0.f); ms[N] = q == 3 ? l8[1] * sgn : 0.f; ev[N] = (q & 1) ? ev[N] : 0.f; });
      cmac5(ev, lo, mr, ms, B5(ev));
    }
    if (m == 1) cmac5(car, car, l32r, l32s, B5(t0));
    cmac5(car, car, pr, ps, B5(ev));
    cmul5(car, car, cr, ci);
    cmac5(o[0], car, lr, ls, B5(bu[0]));
    sfor<1, 8>([&](auto J) { cmac5(o[J], o[J - 1], lr, ls, B5(bu[J])); });
    sfor<0, 5>([&](auto N) { hq[N] = u32x4{pk2(o[0][N], o[1][N]), pk2(o[2][N], o[3][N]), pk2(o[4][N], o[5][N]), pk2(o[6][N], o[7][N])}; });
    LGKM0();
    CBAR();
    sfor<0, 5>([&](auto N) { __builtin_amdgcn_raw_buffer_store_b128(hq[N], rs, ((N & 1) ? ho1 : ho0) + (N >> 1) * 2048, 0, 16); });
    LGKM0();
  }

  {
    const LA bl(blk), w3b[3] = {LA(w3a[0]), LA(w3a[1]), LA(w3a[2])};
    f32x4 acc[4][3], cs[3];
    sfor<0, 3>([&](auto V) { cs[V] = zero4; sfor<0, 4>([&](auto T) { acc[T][V] = zero4; }); });
    bf16x8 fa[2][4], fw[2][3];
    LnG lg[4];
    auto rd = [&](auto S, auto U) {
      sfor<0, 3>([&](auto V) { fw[U][V] = ldtr(AT(w3b[V], BSLOT[S] + U * 12288), AT(w3b[V], BSLOT[S] + U * 12288 + 1536)); });
      sfor<0, 4>([&](auto T) { fa[U][T] = ld128(AT(bl, ASLOT[S] + U * 4096 + T * 512)); });
    };
    auto mm = [&](auto U) {
      sfor<0, 3>([&](auto V) { cs[V] = mfma(fw[U][V], ones, cs[V]); });
      sfor<0, 4>([&](auto T) { sfor<0, 3>([&](auto V) { acc[T][V] = mfma(fw[U][V], fa[U][T], acc[T][V]); }); });
    };
    constexpr std::integral_constant<int, 0> U0{}; constexpr std::integral_constant<int, 1> U1{};
    sfor<30, 40>([&](auto S) {
      CBAR();
      if constexpr (S == 33) FLAG(W_FHH);
      rd(S, U0);
      if constexpr (S > 30) { mm(U1); interleave<15, 10>(); }
      SB();
      rd(S, U1); mm(U0); interleave<15, 10>();
      if constexpr (S == 32) DRAIN();
      LGKM0();
    });
    CBAR();
    sfor<0, 4>([&](auto T) { const f32x4 sv = ldf4(lb + L_ST2 + (T * 16 + c) * 16); lg[T] = LnG{sv[0], sv[1], sv[2], sv[3]}; });
    mm(U1);
    u32x4 hw[3][2];
    const u32 ad = W_HD + t * 196608 + (j * 12 + 3 * w) * 2048 + (16 * (q & 1) + c) * 32 + (q >> 1) * 16;
    sfor<0, 3>([&](auto V) {
      u32x2 hp[4];
      sfor<0, 4>([&](auto T) { hp[T] = ln_gelu4(acc[T][V], cs[V], lg[T]); });
      hw[V][0] = widen(hp[0], hp[1]); hw[V][1] = widen(hp[2], hp[3]);
      sfor<0, 2>([&](auto I) { __builtin_amdgcn_raw_buffer_store_b128(hw[V][I], rs, ad + V * 2048 + I * 1024, 0, 16); });
      SB();
    });
  }

  {
    const u32 cb_ = lb + c * 128 + ((q ^ (c >> 1)) << 4);
    const LA cb0(cb_), cb1(cb_ ^ 64), hl(hb), ha(blk + w * 512), wl(wb);
    f32x4 y[5], z[5];
    sfor<0, 5>([&](auto N) { y[N] = zero4; });
    bf16x8 ft[2][2], fw[2][2][5];
    u32 oo;
    constexpr int S6 = 60 - NL, S7 = 84 - NL;
    auto mm = [&](auto S) {
      constexpr int P = S & 1;
      sfor<0, 2>([&](auto U) { sfor<0, 5>([&](auto N) {
        if constexpr (S >= S6 && S < S7) z[N] = mfma(fw[P][U][N], ft[P][U], z[N]); else y[N] = mfma(fw[P][U][N], ft[P][U], y[N]); }); });
    };
    auto silu = [&](auto N) {
      const f32x4 u = z[N] * -1.4426950409f;
      const f32x4 d = f32x4{__builtin_amdgcn_exp2f(u[0]), __builtin_amdgcn_exp2f(u[1]), __builtin_amdgcn_exp2f(u[2]), __builtin_amdgcn_exp2f(u[3])} + 1.f;
      z[N] *= f32x4{__builtin_amdgcn_rcpf(d[0]), __builtin_amdgcn_rcpf(d[1]), __builtin_amdgcn_rcpf(d[2]), __builtin_amdgcn_rcpf(d[3])};
    };
    sfor<40, 84>([&](auto S) {
      constexpr int P = S & 1;
      constexpr bool g4 = S >= S6 && S < S7;
      CBAR();
      if constexpr (S == 44) FLAG(W_FHD);
      sfor<0, 2>([&](auto U) {
        if constexpr (g4) {
          ft[P][U] = ld128(AT(ha, ASLOT[S] + U * 4096));
          sfor<0, 5>([&](auto N) { fw[P][U][N] = ldtr(AT(wl, BSLOT[S] + U * 5120 + N * 32), AT(wl, BSLOT[S] + U * 5120 + N * 32 + 1280)); });
        } else {
          ft[P][U] = ldtr(AT(hl, ASLOT[S] + U * 4096), AT(hl, ASLOT[S] + U * 4096 + 512));
          sfor<0, 5>([&](auto N) { fw[P][U][N] = ld128(AT((U ? cb1 : cb0), BSLOT[S] + N * 2048)); });
        }
      });
      if constexpr (S > 40) { mm(std::integral_constant<int, S - 1>{}); interleave<10, g4 ? 22 : 14>(); }
      if constexpr (S == 43) DRAIN();
      if constexpr (S == S6 - 1) sfor<0, 5>([&](auto N) { z[N] = f32x4{blo(x1b[N][0]), bhi(x1b[N][0]), blo(x1b[N][1]), bhi(x1b[N][1])}; });
      if constexpr (S >= S6 + 2 && S < S6 + 7) {
        constexpr int N = S - S6 - 2;
        y[N] += (f32x4{blo(xb[N][0]), bhi(xb[N][0]), blo(xb[N][1]), bhi(xb[N][1])} - mu) * rs1 * ldf4(lb + L_DSK + N * 64 + q * 16);
      }
      if constexpr (S == S6 + 8) { oo = ((t * 64 + row) * 640 + j * 80 + q * 4) * 4; PIN(oo); }
      if constexpr (NL > 0 && S >= S7) {
        SB();
        sfor<(5 * (S - S7)) / (NL > 0 ? NL : 1), (5 * (S - S7 + 1)) / (NL > 0 ? NL : 1)>(silu);
      }
      LGKM0();
    });
    asm volatile(".Ls5_code_end:");
    mm(std::integral_constant<int, 83>{});
    const auto ro = __builtin_amdgcn_make_buffer_rsrc(out, 0, 2048 * 640 * 4, 0x00020000);
    sfor<0, 5>([&](auto N) {
      if constexpr (NL == 0) silu(N);
      const f32x4 xv = {blo(xb[N][0]), bhi(xb[N][0]), blo(xb[N][1]), bhi(xb[N][1])};
      __builtin_amdgcn_raw_buffer_store_b128(__builtin_bit_cast(u32x4, xv + y[N] * z[N]), ro, oo + N * 64, 0, 16);
    });
  }
  return;
  }
  {
    const u32 lw = wid - 4;
    asm volatile("" :::
        "v64", "v65", "v66", "v67", "v68", "v69", "v70", "v71", "v72", "v73", "v74", "v75", "v76", "v77", "v78", "v79", "v80", "v81",
        "v82", "v83", "v84", "v85", "v86", "v87", "v88", "v89", "v90", "v91", "v92", "v93", "v94", "v95", "v96", "v97", "v98", "v99",
        "v100", "v101", "v102", "v103", "v104", "v105", "v106", "v107", "v108", "v109", "v110", "v111", "v112", "v113", "v114", "v115",
        "v116", "v117", "v118", "v119", "v120", "v121", "v122", "v123", "v124", "v125", "v126", "v127", "v128", "v129", "v130", "v131",
        "v132", "v133", "v134", "v135", "v136", "v137", "v138", "v139", "v140", "v141", "v142", "v143", "v144", "v145", "v146", "v147",
        "v148", "v149", "v150", "v151", "v152", "v153", "v154", "v155", "v156", "v157", "v158", "v159", "v160", "v161", "v162", "v163",
        "v164", "v165", "v166", "v167", "v168", "v169", "v170", "v171", "v172", "v173", "v174", "v175", "v176", "v177", "v178", "v179",
        "v180", "v181", "v182", "v183", "v184", "v185", "v186", "v187", "v188", "v189", "v190", "v191", "v192", "v193", "v194", "v195",
        "v196", "v197", "v198", "v199", "v200", "v201", "v202", "v203", "v204", "v205", "v206", "v207", "v208", "v209", "v210", "v211",
        "v212", "v213", "v214", "v215", "v216", "v217", "v218", "v219", "v220", "v221", "v222", "v223", "v224", "v225", "v226", "v227",
        "v228", "v229", "v230", "v231", "v232", "v233", "v234", "v235", "v236", "v237", "v238", "v239", "v240", "v241", "v242", "v243");
    const u32 vl = lane * 16, r8 = lane >> 3, c8 = lane & 7;
    u32 vx0, vx1;
    { u32 ra = lw * 16 + r8, rb = ra + 8;
      u32 fa = ((ra >> 1) & 1) | ((ra >> 2) & 2) | ((ra ^ (ra >> 2)) & 4), fb = ((rb >> 1) & 1) | ((rb >> 2) & 2) | ((rb ^ (rb >> 2)) & 4);
      vx0 = ra * 1280 + ((c8 ^ fa) << 4); vx1 = rb * 1280 + ((c8 ^ fb) << 4) - 1024; }
    u32 vw[3];
    #pragma unroll
    for (int i = 0; i < 3; i++) { u32 n = lw * 160 + i * 64 + (i == 2 ? (lane & 31) : lane), pos = n / 10, ch = n % 10;
      u32 k = (pos & ~12u) | ((pos & 4) << 1) | ((pos & 8) >> 1); vw[i] = k * 1280 + ch * 16 - i * 1024; }
    u32 vw0 = vw[0], vw1 = vw[1], vw2 = vw[2];
    const u32 bl_ = lane < 25 ? lane : 24, bx = blockIdx.x;
    u32 pfo = (lw == 0 ? j * 80 + lane : lw == 1 ? (lane < 16 ? j * 80 + 64 + lane : bx * 25 + (lane < 41 ? lane - 16 : 24)) : bx * 25 + bl_) * 128;
    PIN(vx0); PIN(vx1); PIN(vw0); PIN(vw1); PIN(vw2); PIN(pfo);
    const char *gx = (const char*)x, *gw1 = (const char*)w1;
    asm volatile("" : "+s"(gx), "+s"(gw1));
    const char* const w1p = gw1;
    gx += (size_t)t * 81920; gw1 += j * 160;
    LOADER_FIRST();
    asm volatile("global_load_dwordx2 v[248:249], %0, %1 sc1" ::"v"(0u), "s"(ws) : "memory", "v248", "v249");
    LOADER_SECOND();
    const char* pfa1 = (lw == 0 || (lw == 1 && lane < 16) ? gx : lw == 1 ? w1p : (const char*)(lw == 2 ? Bre : Bim)) + pfo;
    LOADER_PROLOGUE();
    const char* pfa2 = lw < 2 ? (const char*)w3 + (size_t)(bx * 60 + lw * 30 + (lane < 30 ? lane : 29)) * 128
                              : (const char*)(lw == 2 ? Cre : Cim) + (size_t)(bx * 25 + bl_) * 128;
    if (lw == 0) *(AS3 volatile u32*)(lb + L_SYNC) = 0;
    const u32 part = lw >> 1;
    const char* gbm = (const char*)(part ? Bim : Bre) + (size_t)(j * 80 + (lw & 1) * 40) * 1280;
    const u32 mbm = (lw & 1) * 10240 + part * 1024;
    const u32 vb0 = r8 * 1280 + ((c8 ^ (part | (r8 & 6))) << 4), vb1 = vb0 + 10240 - 2048, vb2 = vb0 + 20480, vb3 = vb0 + 30720 - 2048, vb4 = vb0 + 40960;
    const char* gh1 = ws + W_H1 + t * 81920 + lw * 2048;
    const char* gx1 = ws + W_X1 + t * 81920 + lw * 2048;
    const char* ghh = ws + W_HH + t * 163840 + lw * 2048;
    const char* gst = ws + W_ST + t * 4096 + lw * 1024;
    const char* ghd = ws + W_HD + t * 196608 + lw * 2048;
    const char* gw2 = ws + W_W2R + j * 102400 + lw * 2560;
    const char* gw4 = ws + W_W4R + j * 245760 + lw * 2560;
    u32x4 ragg; { const u64 ga = (u64)(ws + W_AGG + j * 20480); ragg = u32x4{(u32)ga, (u32)(ga >> 32), 20480u, 0x00020000u}; }
    u32 va[5];
    #pragma unroll
    for (int i = 0; i < 5; i++) { const u32 n = lw * 320 + i * 64 + lane; const int id = t - 1 - (int)(n / 40);
      va[i] = id >= 0 ? id * 640 + (n % 40) * 16 : 0xFFFF0000u; }
    const u32 va0 = va[0], va1 = va[1], va2 = va[2], va3 = va[3], va4 = va[4];
    const char* gw3 = (const char*)w3 + j * 384 + lw * 49152;
    u32 v3[6];
    #pragma unroll
    for (int i = 0; i < 6; i++) { u32 n = i * 64 + lane, k = n / 24, ch = n % 24, vp = ch >> 1, f = ((k >> 1) & 1) | ((k >> 2) & 2);
      u32 v = (vp & ~3u) | ((vp ^ f) & 3); v3[i] = k * 3072 + v * 32 + (ch & 1) * 16 - (i & 3) * 1024; }
    const u32 v30 = v3[0], v31 = v3[1], v32 = v3[2], v33 = v3[3], v34 = v3[4], v35 = v3[5];
    const char* gcr = (const char*)Cre + (size_t)j * 102400;
    const char* gci = (const char*)Cim + (size_t)j * 102400;
    u32 vc[3];
    #pragma unroll
    for (int i = 0; i < 3; i++) { u32 n = lw * 160 + i * 64 + (i == 2 ? (lane & 31) : lane), row = n >> 3;
      vc[i] = row * 1280 + (((n & 7) ^ ((row >> 1) & 7)) << 4) - i * 1024; }
    const u32 vc0 = vc[0], vc1 = vc[1], vc2 = vc[2];
    const u32 pl = lw == 0 ? lane : 0;
    const u32 pf_t = (t * 8 + (pl & 7)) * 128, pf_s = ((pl & 31) * 8 + j) * 128;
    u32 pf_h1; { int tt = t - 1 + (pl < 24 ? pl >> 3 : 1); tt = tt < 0 ? 0 : (tt > 31 ? 31 : tt); pf_h1 = W_FH1 + (tt * 8 + (pl & 7)) * 128; }
    const u32 pf_hd = pl == 8 ? W_FHH + (31 * 8 + j) * 128 : W_FHD + pf_t;
    u32 tlo = 0, thi = 0, pv0, pv1;
    f32x2 sp[8] = {};
    float mn = 0.f, m2 = 0.f;
    asm volatile("s_waitcnt lgkmcnt(0)" ::: "memory");
    LOADER_SCHEDULE();
    LGW();
  }
}

extern "C" void kernel_launch(void* const* d_in, const int* in_sizes, int n_in, void* d_out, int out_size, void* d_ws,
                              size_t ws_size, hipStream_t stream) {
  s5_mixer_kernel<<<256, 512, 0, stream>>>(
      (const u16*)d_in[0], (const u16*)d_in[1], (const u16*)d_in[2], (const u16*)d_in[3], (const u16*)d_in[4],
      (const u16*)d_in[5], (const u16*)d_in[6], (const u16*)d_in[7], (const u16*)d_in[8], (const u16*)d_in[10],
      (const u16*)d_in[11], (const u16*)d_in[12], (const u16*)d_in[13], (float*)d_out, (char*)d_ws);
}
```
